```python
import torch

def _softmax(x, axis=-1):
    return torch.softmax(x, dim=axis)
def _gelu(x):
    return torch.nn.functional.gelu(x, approximate="tanh")
def _silu(x):
    return torch.nn.functional.silu(x)
def _sigmoid(x):
    return torch.sigmoid(x)
def _layernorm(x, axis=-1, eps=1e-5):
    m = x.mean(dim=axis, keepdim=True)
    v = x.var(dim=axis, keepdim=True, unbiased=False)
    return (x - m) / torch.sqrt(v + eps)
def _scatter_add(tgt, idx, src, axis=0):
    return tgt.clone().index_add_(axis, idx, src)
def _segment_sum(src, seg, n):
    out = torch.zeros((n,) + src.shape[1:], dtype=src.dtype)
    return out.index_add_(0, seg, src)
def _pad(x, pw):
    flat = tuple(v for pair in reversed(pw) for v in pair)
    return torch.nn.functional.pad(x, flat)

kernel_name = 'standalone__dense_activation_relu__prompt'

def setup_inputs():

    _w = (lambda a: (lambda t: t.to(torch.bfloat16) if t.is_floating_point() else t)(torch.as_tensor(a)))
    B, S, D, Dff = 1, 2048, 1024, 3584
    return {
        "x": _w((lambda *s: torch.randn(*s, dtype=torch.bfloat16) * 0.05)(B, S, D)),
        "w1": _w((lambda *s: torch.randn(*s, dtype=torch.bfloat16) * 0.05)(D, Dff)),
        "w2": _w((lambda *s: torch.randn(*s, dtype=torch.bfloat16) * 0.05)(Dff, D)),
    }

def reference(x, w1, w2):
    h = torch.einsum("bsd,df->bsf", x, w1)
    zero = torch.zeros_like(h)
    h = torch.maximum(h, zero)
    return torch.einsum("bsf,fd->bsd", h, w2)
```

```cpp
#include <hip/hip_runtime.h>
#include <hip/hip_bf16.h>

#define LAS __attribute__((address_space(3)))
#define GAS __attribute__((address_space(1)))
#define DI __device__ __forceinline__
typedef __bf16 bf16x8 __attribute__((ext_vector_type(8)));
typedef __bf16 bf16x4 __attribute__((ext_vector_type(4)));
typedef __bf16 bf16x2 __attribute__((ext_vector_type(2)));
typedef float f32x4 __attribute__((ext_vector_type(4)));
typedef float f32x2 __attribute__((ext_vector_type(2)));
typedef unsigned u32x4 __attribute__((ext_vector_type(4)));
typedef unsigned u32x2 __attribute__((ext_vector_type(2)));
typedef unsigned long long u64;

constexpr int M_ = 2048, D_ = 1024, F_ = 3584;
constexpr unsigned X_BYTES = M_ * D_ * 2, W_BYTES = D_ * F_ * 2, H_BYTES = M_ * F_ * 2, O_BYTES = M_ * D_ * 2;
constexpr unsigned XCH_BYTES = 256u * 32768u;
constexpr unsigned WS_H = 0, WS_XCH = H_BYTES, WS_FLAG = H_BYTES + XCH_BYTES;

constexpr int G1_STAGES = 3, G1_STAGE_B = 44 * 1024, G1_NKT = 16;
constexpr int G2_STAGES = 4, G2_STAGE_B = 32 * 1024, G2_NKT = 28;

#if defined(__HIP_DEVICE_COMPILE__)
template <int N> DI void waitv() { asm volatile("s_waitcnt vmcnt(%0)" ::"n"(N) : "memory"); }
DI void waitl0() { asm volatile("s_waitcnt lgkmcnt(0)" ::: "memory"); }
DI void bar() { asm volatile("" ::: "memory"); __builtin_amdgcn_s_barrier(); asm volatile("" ::: "memory"); }
DI bf16x8 lds128(unsigned a) { return *(const LAS bf16x8*)a; }
DI bf16x4 ldstr(unsigned a) { return __builtin_amdgcn_ds_read_tr16_b64_v4bf16((LAS bf16x4*)a); }
DI bf16x8 cat(bf16x4 a, bf16x4 b) { return __builtin_shufflevector(a, b, 0, 1, 2, 3, 4, 5, 6, 7); }
DI unsigned pk(float lo, float hi) { f32x2 v = {lo, hi}; return __builtin_bit_cast(unsigned, __builtin_convertvector(v, bf16x2)); }
DI float relu(float v) { return __builtin_elementwise_maximum(v, 0.0f); }
#define SGB(mask, n) __builtin_amdgcn_sched_group_barrier(mask, n, 0)
#define STAMP(ws, k, i) do {} while (0)
#endif

__global__ __launch_bounds__(512, 2) void g1_kernel(const void* __restrict__ x, const void* __restrict__ w1, char* __restrict__ ws) {
#if defined(__HIP_DEVICE_COMPILE__)
  const int wid = __builtin_amdgcn_readfirstlane(threadIdx.x >> 6), lane = threadIdx.x & 63;
  const int bid = blockIdx.x, xcd = bid & 7, idx = bid >> 3;
  const int pm = (xcd & 3) * 4 + (idx & 3), pn = (xcd >> 2) * 8 + (idx >> 2);
  if (wid == 0) STAMP(ws, 0, 0);
  if (wid >= 4) {
    const int lw = wid - 4;
    const auto rx = __builtin_amdgcn_make_buffer_rsrc((void*)x, 0, X_BYTES, 0x00020000);
    const auto rw = __builtin_amdgcn_make_buffer_rsrc((void*)w1, 0, W_BYTES, 0x00020000);
    unsigned va[4], vb[7];
#pragma unroll
    for (int i = 0; i < 4; ++i) { const int row = (lw * 4 + i) * 8 + (lane >> 3); va[i] = (pm * 128 + row) * 2048 + (((lane & 7) ^ ((row >> 1) & 7)) << 4); }
#pragma unroll
    for (int i = 0; i < 7; ++i) { const int b = (lw * 7 + i) * 1024 + lane * 16, row = b / 448, cb = b - row * 448; vb[i] = row * 7168 + pn * 448 + (cb ^ (((row >> 3) & 1) << 5)); }
    auto issue = [&](int t, unsigned slot) {
      const unsigned sa = t * 128, sb = t * (64 * 7168);
#pragma unroll
      for (int i = 0; i < 4; ++i) __builtin_amdgcn_raw_ptr_buffer_load_lds(rx, (LAS void*)(slot + (lw * 4 + i) * 1024), 16, va[i], sa, 0, 0);
#pragma unroll
      for (int i = 0; i < 7; ++i) __builtin_amdgcn_raw_ptr_buffer_load_lds(rw, (LAS void*)(slot + 16384 + (lw * 7 + i) * 1024), 16, vb[i], sb, 0, 0);
    };
    issue(0, 0);
    issue(1, G1_STAGE_B);
    if (lane == 0) *(unsigned*)(ws + WS_FLAG + (bid * 4 + lw) * 128) = 0u;
    unsigned slot = 2 * G1_STAGE_B;
    waitv<12>();
    bar();
    for (int t = 0; t < G1_NKT - 2; ++t) {
      issue(t + 2, slot);
      slot = slot == 2 * G1_STAGE_B ? 0 : slot + G1_STAGE_B;
      waitv<11>();
      bar();
    }
    waitv<0>();
    bar();
    return;
  }
  __builtin_amdgcn_s_setprio(2);
  const int wr = wid >> 1, wc = wid & 1, fr = lane & 15, g = lane >> 4, q = fr >> 2, p = lane & 3;
  unsigned xa[2], wa[2];
#pragma unroll
  for (int ks = 0; ks < 2; ++ks) xa[ks] = (wr * 64 + fr) * 128 + (((4 * ks + g) ^ (fr >> 1)) << 4);
#pragma unroll
  for (int e = 0; e < 2; ++e) wa[e] = 16384 + (8 * g + q) * 448 + wc * 224 + 8 * p + ((((wc + e) & 1) == 0) ? 32 * (g & 1) : -32 * (g & 1));
  f32x4 acc[4][7];
#pragma unroll
  for (int i = 0; i < 4; ++i)
#pragma unroll
    for (int j = 0; j < 7; ++j) acc[i][j] = (f32x4){0.f, 0.f, 0.f, 0.f};
  bf16x8 xf[2][4], wf[2][7];
  auto rd = [&](int ks, unsigned slot) {
#pragma unroll
    for (int i = 0; i < 4; ++i) xf[ks][i] = lds128(slot + xa[ks] + i * 2048);
#pragma unroll
    for (int j = 0; j < 7; ++j) wf[ks][j] = cat(ldstr(slot + wa[j & 1] + j * 32 + ks * 32 * 448), ldstr(slot + wa[j & 1] + j * 32 + (ks * 32 + 4) * 448));
  };
  auto mm = [&](int ks) {
#pragma unroll
    for (int j = 0; j < 7; ++j)
#pragma unroll
      for (int i = 0; i < 4; ++i) acc[i][j] = __builtin_amdgcn_mfma_f32_16x16x32_bf16(wf[ks][j], xf[ks][i], acc[i][j], 0, 0, 0);
  };
  auto sched = [&]() {
#pragma unroll
    for (int i = 0; i < 18; ++i) { SGB(0x8, 1); SGB(0x100, 1); }
    SGB(0x8, 10);
  };
  bar();
  if (wid == 0) STAMP(ws, 0, 1);
  rd(0, 0);
  unsigned slot = 0;
  for (int t = 0; t < G1_NKT - 1; ++t) {
    rd(1, slot); mm(0); sched();
    waitl0();
    bar();
    slot = slot == 2 * G1_STAGE_B ? 0 : slot + G1_STAGE_B;
    rd(0, slot); mm(1); sched();
  }
  rd(1, slot); mm(0); sched();
  mm(1);
  if (wid == 0) STAMP(ws, 0, 2);
  char* hb = ws + WS_H + ((size_t)(pm * 8 + wr * 4) * 112 + pn * 7) * 1024 + lane * 16;
#pragma unroll
  for (int i = 0; i < 4; ++i) {
    unsigned w[7][2];
#pragma unroll
    for (int j = 0; j < 7; ++j) { w[j][0] = pk(relu(acc[i][j][0]), relu(acc[i][j][1])); w[j][1] = pk(relu(acc[i][j][2]), relu(acc[i][j][3])); }
    char* hr = hb + i * 112 * 1024;
    if (wc == 0) {
#pragma unroll
      for (int s = 0; s < 3; ++s) *(u32x4*)(hr + s * 1024) = (u32x4){w[2 * s][0], w[2 * s][1], w[2 * s + 1][0], w[2 * s + 1][1]};
      *(u32x2*)(hr + 3 * 1024) = (u32x2){w[6][0], w[6][1]};
    } else {
      *(u32x2*)(hr + 3 * 1024 + 8) = (u32x2){w[0][0], w[0][1]};
#pragma unroll
      for (int s = 0; s < 3; ++s) *(u32x4*)(hr + (4 + s) * 1024) = (u32x4){w[2 * s + 1][0], w[2 * s + 1][1], w[2 * s + 2][0], w[2 * s + 2][1]};
    }
  }
#endif
}

__global__ __launch_bounds__(512, 2) void g2_kernel(const void* __restrict__ w2, char* __restrict__ ws, void* __restrict__ out) {
#if defined(__HIP_DEVICE_COMPILE__)
  const int wid = __builtin_amdgcn_readfirstlane(threadIdx.x >> 6), lane = threadIdx.x & 63;
  const int bid = blockIdx.x, xcd = bid & 7, idx = bid >> 3;
  const int pm = (xcd & 3) * 4 + (idx & 3), pn = idx >> 2, kh = xcd >> 2;
  if (wid == 0) STAMP(ws, 1, 0);
  if (wid >= 4) {
    const int lw = wid - 4;
    const auto rh = __builtin_amdgcn_make_buffer_rsrc((void*)(ws + WS_H), 0, H_BYTES, 0x00020000);
    const auto rw = __builtin_amdgcn_make_buffer_rsrc((void*)w2, 0, W_BYTES, 0x00020000);
    unsigned va[4], vb[4];
#pragma unroll
    for (int i = 0; i < 4; ++i) { const int pc = lw * 4 + i; va[i] = ((pm * 8 + (pc >> 1)) * 112 + kh * 56 + (pc & 1)) * 1024 + lane * 16; }
#pragma unroll
    for (int i = 0; i < 4; ++i) { const int row = (lw * 4 + i) * 4 + (lane >> 4); vb[i] = (kh * 1792 + row) * 2048 + pn * 256 + (((lane & 15) << 4) ^ ((row & 7) << 5)); }
    auto issue = [&](int t, unsigned slot) {
      const unsigned sa = t * 2048, sb = t * (64 * 2048);
#pragma unroll
      for (int i = 0; i < 4; ++i) __builtin_amdgcn_raw_ptr_buffer_load_lds(rh, (LAS void*)(slot + (lw * 4 + i) * 1024), 16, va[i], sa, 0, 0);
#pragma unroll
      for (int i = 0; i < 4; ++i) __builtin_amdgcn_raw_ptr_buffer_load_lds(rw, (LAS void*)(slot + 16384 + (lw * 4 + i) * 1024), 16, vb[i], sb, 0, 0);
    };
    issue(0, 0);
    issue(1, G2_STAGE_B);
    issue(2, 2 * G2_STAGE_B);
    unsigned slot = 3 * G2_STAGE_B;
    waitv<16>();
    bar();
    for (int t = 0; t < G2_NKT - 3; ++t) {
      issue(t + 3, slot);
      slot = (slot + G2_STAGE_B) & (4 * G2_STAGE_B - 1);
      waitv<16>();
      bar();
    }
    waitv<8>();
    bar();
    waitv<0>();
    bar();
    return;
  }
  __builtin_amdgcn_s_setprio(2);
  const int wr = wid >> 1, wc = wid & 1, fr = lane & 15, g = lane >> 4, q = fr >> 2, p = lane & 3;
  const unsigned ha = wr * 8192 + lane * 16;
  unsigned wa[4];
#pragma unroll
  for (int j = 0; j < 4; ++j) wa[j] = 16384 + (4 * g + q) * 256 + (((wc * 4 + j) ^ ((4 * g + q) & 7)) << 5) + 8 * p;
  f32x4 acc[4][4];
#pragma unroll
  for (int i = 0; i < 4; ++i)
#pragma unroll
    for (int j = 0; j < 4; ++j) acc[i][j] = (f32x4){0.f, 0.f, 0.f, 0.f};
  bf16x8 hf[2][4], wf[2][4];
  auto rd = [&](int ks, unsigned slot) {
#pragma unroll
    for (int i = 0; i < 4; ++i) hf[ks][i] = lds128(slot + ha + i * 2048 + ks * 1024);
#pragma unroll
    for (int j = 0; j < 4; ++j) wf[ks][j] = cat(ldstr(slot + wa[j] + ks * 32 * 256), ldstr(slot + wa[j] + (ks * 32 + 16) * 256));
  };
  auto mm = [&](int ks) {
#pragma unroll
    for (int j = 0; j < 4; ++j)
#pragma unroll
      for (int i = 0; i < 4; ++i) acc[i][j] = __builtin_amdgcn_mfma_f32_16x16x32_bf16(wf[ks][j], hf[ks][i], acc[i][j], 0, 0, 0);
  };
  auto sched = [&]() {
#pragma unroll
    for (int i = 0; i < 12; ++i) { SGB(0x8, 1); SGB(0x100, 1); }
    SGB(0x8, 4);
  };
  bar();
  if (wid == 0) STAMP(ws, 1, 1);
  rd(0, 0);
  unsigned slot = 0;
  for (int t = 0; t < G2_NKT - 1; ++t) {
    rd(1, slot); mm(0); sched();
    waitl0();
    bar();
    slot = (slot + G2_STAGE_B) & (4 * G2_STAGE_B - 1);
    rd(0, slot); mm(1); sched();
  }
  rd(1, slot); mm(0); sched();
  mm(1);
  if (wid == 0) STAMP(ws, 1, 2);
  const auto rxc = __builtin_amdgcn_make_buffer_rsrc((void*)(ws + WS_XCH), 0, XCH_BYTES, 0x00020000);
  const unsigned mine = (bid * 4 + wid) * 8192 + lane * 16, theirs = ((bid ^ 4) * 4 + wid) * 8192 + lane * 16;
  f32x4 keep[2][4];
#pragma unroll
  for (int i = 0; i < 2; ++i)
#pragma unroll
    for (int j = 0; j < 4; ++j) {
      const f32x4 send = kh ? acc[i][j] : acc[2 + i][j];
      keep[i][j] = kh ? acc[2 + i][j] : acc[i][j];
      __builtin_amdgcn_raw_buffer_store_b128(__builtin_bit_cast(u32x4, send), rxc, mine + (i * 4 + j) * 1024, 0, 16);
    }
  waitv<0>();
  GAS unsigned* fl = (GAS unsigned*)(ws + WS_FLAG);
  if (lane == 0) __hip_atomic_store(fl + (bid * 4 + wid) * 32, 1u, __ATOMIC_RELAXED, __HIP_MEMORY_SCOPE_AGENT);
  if (wid == 0) STAMP(ws, 1, 3);
  {
    GAS unsigned* pf = fl + ((bid ^ 4) * 4 + wid) * 32;
    for (unsigned spins = 0; spins < (1u << 22); ++spins) {
      if (__builtin_amdgcn_readfirstlane(__hip_atomic_load(pf, __ATOMIC_RELAXED, __HIP_MEMORY_SCOPE_AGENT)) == 1u) break;
      __builtin_amdgcn_s_sleep(1);
    }
    asm volatile("" ::: "memory");
  }
  if (wid == 0) STAMP(ws, 1, 4);
  char* ob = (char*)out + (size_t)(pm * 128 + wr * 64 + kh * 32 + fr) * 2048 + (pn * 128 + wc * 64 + 4 * g) * 2;
  u32x4 got[2][4];
#pragma unroll
  for (int i = 0; i < 2; ++i)
#pragma unroll
    for (int j = 0; j < 4; ++j) got[i][j] = __builtin_amdgcn_raw_buffer_load_b128(rxc, theirs + (i * 4 + j) * 1024, 0, 16);
#pragma unroll
  for (int i = 0; i < 2; ++i)
#pragma unroll
    for (int j = 0; j < 4; ++j) {
      const f32x4 o = __builtin_bit_cast(f32x4, got[i][j]);
      const f32x4 s = kh ? o + keep[i][j] : keep[i][j] + o;
      *(u32x2*)(ob + i * 16 * 2048 + j * 32) = (u32x2){pk(s[0], s[1]), pk(s[2], s[3])};
    }
#endif
}

extern "C" void kernel_launch(void* const* d_in, const int* in_sizes, int n_in, void* d_out, int out_size, void* d_ws, size_t ws_size,
                              hipStream_t stream) {
  (void)in_sizes; (void)n_in; (void)out_size; (void)ws_size;
  (void)hipFuncSetAttribute((const void*)g1_kernel, hipFuncAttributeMaxDynamicSharedMemorySize, G1_STAGES * G1_STAGE_B);
  (void)hipFuncSetAttribute((const void*)g2_kernel, hipFuncAttributeMaxDynamicSharedMemorySize, G2_STAGES * G2_STAGE_B);
  g1_kernel<<<256, 512, G1_STAGES * G1_STAGE_B, stream>>>(d_in[0], d_in[1], (char*)d_ws);
  g2_kernel<<<256, 512, G2_STAGES * G2_STAGE_B, stream>>>(d_in[2], (char*)d_ws, d_out);
}
```

```cpp
#include <hip/hip_runtime.h>
#include <hip/hip_bf16.h>

#define LAS __attribute__((address_space(3)))
#define GAS __attribute__((address_space(1)))
#define DI __device__ __forceinline__
typedef __bf16 bf16x8 __attribute__((ext_vector_type(8)));
typedef __bf16 bf16x4 __attribute__((ext_vector_type(4)));
typedef __bf16 bf16x2 __attribute__((ext_vector_type(2)));
typedef float f32x4 __attribute__((ext_vector_type(4)));
typedef float f32x2 __attribute__((ext_vector_type(2)));
typedef unsigned u32x4 __attribute__((ext_vector_type(4)));
typedef unsigned u32x2 __attribute__((ext_vector_type(2)));
typedef unsigned long long u64;

constexpr int M_ = 2048, D_ = 1024, F_ = 3584;
constexpr unsigned X_BYTES = M_ * D_ * 2, W_BYTES = D_ * F_ * 2, H_BYTES = M_ * F_ * 2;
constexpr unsigned XCH_BYTES = 256u * 32768u;
constexpr unsigned WS_H = 0, WS_XCH = H_BYTES;
constexpr unsigned WS_XFLAG = WS_XCH + XCH_BYTES;
constexpr unsigned WS_HFLAG = WS_XFLAG + 256 * 4 * 128;
constexpr unsigned WS_EPOCH = WS_HFLAG + 256 * 256;

#define G1ROT (4 * (xcd >> 1) + 8 * (xcd & 1) + 2 * (lm >> 2) + 2 * (ln >> 1))
#define PFW2 9
#define ORDER 1
#define ORDSTART (2 * ln)
#define PFL2A -1
#define PFL2B -1
#define POLLDELAY 0
#define POLLSLEEP 1
#define FLAG1AT 0
#define FLAG2AT 2
constexpr unsigned G1_STAGE_B = 44 * 1024;
constexpr unsigned ST = 16384, B_RING = 5 * ST, LDS_BYTES = 10 * ST;
constexpr unsigned SPIN_MAX = 1u << 16;

#if defined(__HIP_DEVICE_COMPILE__)
template <int N> DI void waitv() { asm volatile("s_waitcnt vmcnt(%0)" ::"n"(N) : "memory"); }
DI void waitl0() { asm volatile("" ::: "memory"); __builtin_amdgcn_s_waitcnt(0xc07f); asm volatile("" ::: "memory"); }
DI void bar() { asm volatile("" ::: "memory"); __builtin_amdgcn_s_barrier(); asm volatile("" ::: "memory"); }
DI void pin() { __builtin_amdgcn_sched_barrier(0); }
DI bf16x8 lds128(unsigned a) { return *(const LAS bf16x8*)a; }
DI bf16x4 ldstr(unsigned a) { return __builtin_amdgcn_ds_read_tr16_b64_v4bf16((LAS bf16x4*)a); }
DI bf16x8 cat(bf16x4 a, bf16x4 b) { return __builtin_shufflevector(a, b, 0, 1, 2, 3, 4, 5, 6, 7); }
DI unsigned pk(float lo, float hi) { f32x2 v = {lo, hi}; return __builtin_bit_cast(unsigned, __builtin_convertvector(v, bf16x2)); }
DI float relu(float v) { return __builtin_elementwise_maximum(v, 0.0f); }
DI u64 ld_agent(const char* p) { return __hip_atomic_load((GAS u64*)p, __ATOMIC_RELAXED, __HIP_MEMORY_SCOPE_AGENT); }
DI void st_agent(char* p, u64 v) { __hip_atomic_store((GAS u64*)p, v, __ATOMIC_RELAXED, __HIP_MEMORY_SCOPE_AGENT); }
DI u64 uni(u64 v) { return ((u64)(unsigned)__builtin_amdgcn_readfirstlane((unsigned)(v >> 32)) << 32) | (unsigned)__builtin_amdgcn_readfirstlane((unsigned)v); }
#define MFMA(c, a, b) asm volatile("v_mfma_f32_16x16x32_bf16 %0, %1, %2, %0" : "+v"(c) : "v"(a), "v"(b))
#define MFMA0(c, a, b) asm volatile("v_mfma_f32_16x16x32_bf16 %0, %1, %2, 0" : "=&v"(c) : "v"(a), "v"(b))
#define DMA(rs, lds, vo, so, aux) __builtin_amdgcn_raw_ptr_buffer_load_lds(rs, (LAS void*)(lds), 16, vo, so, 0, aux)
#define STAMP(k, i) do {} while (0)
#define STAMP_DECL
#define STAMP_FLUSH(mask) do {} while (0)
#endif

__global__ __launch_bounds__(512, 2) void mlp_kernel(const void* __restrict__ x, const void* __restrict__ w1, const void* __restrict__ w2,
                                                     char* __restrict__ ws, void* __restrict__ out) {
#if defined(__HIP_DEVICE_COMPILE__)
  const int wid = __builtin_amdgcn_readfirstlane(threadIdx.x >> 6), lane = threadIdx.x & 63;
  const int bid = blockIdx.x, xcd = bid & 7, idx = bid >> 3;
  const int lm = idx & 7, ln = idx >> 3, pm = (xcd & 1) * 8 + lm, pn = (xcd >> 1) * 4 + ln, kh = pn >> 3, own = pn & 7;
  STAMP_DECL;
  STAMP(0, 0);
  const u64 epoch = ld_agent(ws + WS_EPOCH + pm * 128);
  if (wid >= 4) {
    const int lw = wid - 4;
    const auto rx = __builtin_amdgcn_make_buffer_rsrc((void*)x, 0, X_BYTES, 0x00020000);
    const auto rw1 = __builtin_amdgcn_make_buffer_rsrc((void*)w1, 0, W_BYTES, 0x00020000);
    const auto rw2 = __builtin_amdgcn_make_buffer_rsrc((void*)w2, 0, W_BYTES, 0x00020000);
    const auto rh = __builtin_amdgcn_make_buffer_rsrc((void*)(ws + WS_H), 0, H_BYTES, 0x00020000);
    u64 token;
    {
      const int rot = G1ROT;
      unsigned va[4], vb[7];
#pragma unroll
      for (int i = 0; i < 4; ++i) { const int row = (lw * 4 + i) * 8 + (lane >> 3); va[i] = (pm * 128 + row) * 2048 + (((lane & 7) ^ ((row >> 1) & 7)) << 4); }
#pragma unroll
      for (int i = 0; i < 7; ++i) { const int b = (lw * 7 + i) * 1024 + lane * 16, row = b / 448, cb = b - row * 448; vb[i] = row * 7168 + pn * 448 + (cb ^ (((row >> 3) & 1) << 5)); }
      auto issue = [&](int t, unsigned slot) {
        const unsigned kt = (t + rot) & 15, sa = kt * 128, sb = kt * (64 * 7168);
#pragma unroll
        for (int i = 0; i < 4; ++i) DMA(rx, slot + (lw * 4 + i) * 1024, va[i], sa, 0);
#pragma unroll
        for (int i = 0; i < 7; ++i) DMA(rw1, slot + 16384 + (lw * 7 + i) * 1024, vb[i], sb, 0);
      };
      issue(0, 0);
      issue(1, G1_STAGE_B);
      token = uni(epoch) * 6364136223846793005ull + 1442695040888963407ull;
      asm volatile("" ::"s"(token));
      unsigned slot = 2 * G1_STAGE_B;
      waitv<11>();
      bar();
      for (int t = 0; t < 14; ++t) {
        if (t == PFW2) {
          const unsigned o = lane < 56 ? ((bid * 4 + lw) * 56 + lane) * 128 : 0xfffffff0u;
          __builtin_amdgcn_raw_ptr_buffer_load_lds(rw2, (LAS void*)(3 * G1_STAGE_B + lw * 256), 4, o, 0, 0, 0);
        }
        if (t == PFL2A || t == PFL2B) {
          const unsigned o = lane < 56 ? (kh * 1792 + idx * 56 + lw * 14 + (t == PFL2B) * 7 + (lane >> 3)) * 2048 + (own >> 2) * 1024 + (lane & 7) * 128 : 0xfffffff0u;
          __builtin_amdgcn_raw_ptr_buffer_load_lds(rw2, (LAS void*)(3 * G1_STAGE_B + lw * 256), 4, o, 0, 0, 0);
        }
        issue(t + 2, slot);
        slot = slot == 2 * G1_STAGE_B ? 0 : slot + G1_STAGE_B;
        waitv<11>();
        bar();
      }
      waitv<0>();
      bar();
    }
    unsigned va[4], vb[4];
#pragma unroll
    for (int i = 0; i < 4; ++i) va[i] = ((pm * 8 + (lw & 1) * 4 + i) * 112 + kh * 56) * 1024 + lane * 16;
#pragma unroll
    for (int i = 0; i < 4; ++i) { const int row = i * 4 + (lane >> 4); vb[i] = (kh * 1792 + row) * 2048 + own * 256 + (((lane & 15) << 4) ^ ((row & 7) << 5)); }
    unsigned tab;
    {
      const int s = 2 * lane + (lw >> 1), k = s < 21 ? (s - 7) >> 1 : s < 35 ? (s - 21) >> 1 : ((s - 35) * 86) >> 8;
      const int pr = s < 7 ? own : (own ^ (k + 1)) & 7;
      const int r = s < 7 ? s : s < 21 ? (s - 7) & 1 : s < 35 ? 2 + ((s - 21) & 1) : 4 + (s - 35) - 3 * k;
      const int cb = r < 6 ? 7 * (r & 1) + (r & ~1) + (lw & 1) : 6 + 7 * (lw & 1);
      tab = (pr * 7 + r) | (pr * 14 + cb) << 8 | pr * 8 << 16 | (s < 7 ? 0u : r < 2 ? 0x01u : r < 4 ? 0x50u : 0xa0u) << 24;
    }
    auto entry = [&](int T) { return (unsigned)__builtin_amdgcn_readlane(tab, T); };
    auto issueA = [&](int T, unsigned slot) {
      const unsigned so = (entry(T) & 0xff) * 1024;
#pragma unroll
      for (int i = 0; i < 4; ++i) DMA(rh, slot + (((lw & 1) * 4 + i) * 2 + (lw >> 1)) * 1024, va[i], so, 16);
    };
    auto issueB = [&](int T, unsigned slot) {
      const unsigned so = ((entry(T) >> 8) & 0xff) * (16 * 2048);
#pragma unroll
      for (int i = 0; i < 4; ++i) DMA(rw2, B_RING + slot + (lw * 4 + i) * 1024, vb[i], so, 0);
    };
    const char* fp = ws + WS_HFLAG + (pm * 16 + kh * 8 + (lane >> 3)) * 256 + ((lane & 4) ? 128 + (lw & 1) * 32 + (lane & 3) * 8 : 0);
    char* const myflag = ws + WS_HFLAG + (pm * 16 + pn) * 256;
    u64 ready = 0;
    auto needof = [&](int T) { const unsigned e = entry(T); return (u64)(e >> 24) << ((e >> 16) & 0xff); };
    auto ensure = [&](u64 need, bool all) {
      if (__builtin_expect((ready & need) != need, 0)) {
        for (unsigned spins = 0;; ++spins) {
          bool hit = false;
          if ((all && spins == 0) || (((need & ~ready) >> lane) & 1)) hit = ld_agent(fp) == token;
          ready |= __builtin_amdgcn_ballot_w64(hit);
          if (spins >= SPIN_MAX) ready = ~0ull;
          if ((ready & need) == need) break;
          __builtin_amdgcn_s_sleep(POLLSLEEP);
        }
      }
      asm volatile("" ::: "memory");
    };
    auto issue = [&](int T, unsigned slot) { ensure(needof(T), true); issueA(T, slot); issueB(T, slot); };
#pragma unroll
    for (int T = 0; T < 5; ++T) issueB(T, T * ST);
    const unsigned pw = 4 * ST + ((lw & 1) * 8 + (lw >> 1)) * 1024;
    if (lane == 0) *(LAS u32x4*)pw = (u32x4){0u, 0u, 0u, 0u};
    bar();
    waitl0();
    waitv<4>();
    bar();
    if (lw == 0 && lane == 0) st_agent(myflag, token);
    u32x4 hv[5][2];
#pragma unroll
    for (int r = 2; r < 7; ++r)
#pragma unroll
      for (int e = 0; e < 2; ++e) hv[r - 2][e] = *(const LAS u32x4*)((r >> 1) * ST + ((lw * 2 + e) * 2 + (r & 1)) * 1024 + lane * 16);
    const unsigned hb = ((pm * 8 + lw * 2) * 112 + pn * 7) * 1024 + lane * 16;
    auto publish = [&](int r) {
#pragma unroll
      for (int e = 0; e < 2; ++e) __builtin_amdgcn_raw_buffer_store_b128(hv[r - 2][e], rh, hb + (e * 112 + r) * 1024, 0, 16);
    };
    waitl0();
    ensure(needof(3) | needof(4) | needof(5), false);
    STAMP(0, 4);
    if (lw >= 2) issueA(3, 3 * ST);
    for (;;) {
      const u32x4 v = *(volatile const LAS u32x4*)pw;
      if (__builtin_amdgcn_readfirstlane(v[0] & v[1] & v[2] & v[3])) break;
    }
    asm volatile("" ::: "memory");
    STAMP(0, 5);
    issueA(4, 4 * ST);
    issueA(5, 0);
    issueB(5, 0);
    issueB(6, ST);
    publish(2); publish(3);
    waitv<20>();
    bar();
    ensure(needof(6) | needof(7), true);
    issueA(6, ST);
    issueA(7, 2 * ST); issueB(7, 2 * ST);
    waitv<28>();
    bar();
    issue(8, 3 * ST);
    waitv<20>();
    if (lane == 0) st_agent(myflag + 128 + lw * 16, token);
    bar();
    publish(4);
    issue(9, 4 * ST);
    waitv<26>();
    bar();
    publish(5);
    waitv<20>();
    bar();
    publish(6);
    issue(10, 0);
    issue(11, ST);
    waitv<30>();
    bar();
    issue(12, 2 * ST);
    waitv<28>();
    bar();
    issue(13, 3 * ST);
    waitv<24>();
    if (lane == 0) st_agent(myflag + 128 + lw * 16 + 8, token);
    bar();
    unsigned slot = 4 * ST;
    for (int T = 14; T < 28; ++T) {
      issue(T, slot);
      slot = slot == 4 * ST ? 0 : slot + ST;
      waitv<24>();
      bar();
    }
    waitv<16>();
    bar();
    waitv<8>();
    bar();
    waitv<0>();
    bar();
    if (lw == 2) STAMP_FLUSH(0x0030);
    return;
  }
  __builtin_amdgcn_s_setprio(2);
  const int wr = wid >> 1, wc = wid & 1, fr = lane & 15, g = lane >> 4, q = fr >> 2, p = lane & 3;
  const u64 token = uni(epoch) * 6364136223846793005ull + 1442695040888963407ull;
  asm volatile("" ::"s"(token));
  const auto rh = __builtin_amdgcn_make_buffer_rsrc((void*)(ws + WS_H), 0, H_BYTES, 0x00020000);
  const unsigned hg = ((pm * 8 + wr * 4) * 112 + pn * 7) * 1024 + lane * 16;
  {
    unsigned xa[2], wa[2];
#pragma unroll
    for (int ks = 0; ks < 2; ++ks) xa[ks] = (wr * 64 + fr) * 128 + (((4 * ks + g) ^ (fr >> 1)) << 4);
#pragma unroll
    for (int e = 0; e < 2; ++e) wa[e] = 16384 + (8 * g + q) * 448 + wc * 224 + 8 * p + ((((wc + e) & 1) == 0) ? 32 * (g & 1) : -32 * (g & 1));
    f32x4 acc[4][7];
#pragma unroll
    for (int i = 0; i < 4; ++i)
#pragma unroll
      for (int j = 0; j < 7; ++j) { acc[i][j] = (f32x4){0.f, 0.f, 0.f, 0.f}; asm volatile("" : "+v"(acc[i][j])); }
    bf16x8 xf[2][4], wf[2][7];
    auto seg = [&](int c, int ks, unsigned slot, bool doread) {
      bf16x4 lo[7];
#pragma unroll
      for (int m = 0; m < 28; ++m) {
        MFMA(acc[m & 3][m >> 2], wf[c][m >> 2], xf[c][m & 3]);
        pin();
        if (doread && m < 18) {
          if (m >= 2 && m < 6) xf[c ^ 1][m - 2] = lds128(slot + xa[ks] + (m - 2) * 2048);
          else {
            const int jj = m < 2 ? 0 : (m - 4) >> 1;
            if ((m & 1) == 0) lo[jj] = ldstr(slot + wa[jj & 1] + jj * 32 + ks * 32 * 448);
            else wf[c ^ 1][jj] = cat(lo[jj], ldstr(slot + wa[jj & 1] + jj * 32 + (ks * 32 + 4) * 448));
          }
        }
        pin();
      }
    };
    bar();
    STAMP(0, 1);
#pragma unroll
    for (int i = 0; i < 4; ++i) xf[0][i] = lds128(xa[0] + i * 2048);
#pragma unroll
    for (int j = 0; j < 7; ++j) wf[0][j] = cat(ldstr(wa[j & 1] + j * 32), ldstr(wa[j & 1] + j * 32 + 4 * 448));
    for (int t = 0; t < 5; ++t) {
#pragma unroll
      for (int u = 0; u < 3; ++u) {
        seg(0, 1, u * G1_STAGE_B, true);
        waitl0();
        bar();
        pin();
        seg(1, 0, ((u + 1) % 3) * G1_STAGE_B, true);
      }
    }
    seg(0, 1, 0, true);
    waitl0();
    bar();
    pin();
    STAMP(0, 2);
    const unsigned hl = wr * 8192 + wc * 1024 + lane * 16;
    u32x2 hp[4][7];
    auto fin = [&](int n) {
      const int i = n & 3, j = n >> 2;
      const f32x4 v = acc[i][j];
      hp[i][j] = (u32x2){pk(relu(v[0]), relu(v[1])), pk(relu(v[2]), relu(v[3]))};
      if (j == 6) *(LAS u32x2*)(wr * 8192 + lane * 16 + wc * 8 + 3 * ST + i * 2048) = hp[i][6];
      else if (j & 1) {
        const u32x4 w = {hp[i][j - 1][0], hp[i][j - 1][1], hp[i][j][0], hp[i][j][1]};
        if (j == 1) __builtin_amdgcn_raw_buffer_store_b128(w, rh, hg + wc * 1024 + i * 112 * 1024, 0, 16);
        *(LAS u32x4*)(hl + (j >> 1) * ST + i * 2048) = w;
      }
    };
#pragma unroll
    for (int m = 0; m < 28; ++m) {
      MFMA(acc[m & 3][m >> 2], wf[1][m >> 2], xf[1][m & 3]);
      pin();
      if (m >= 3) fin(m - 3);
      pin();
    }
    asm volatile("s_nop 7" ::: "memory");
    pin();
    fin(25); fin(26); fin(27);
  }
  const unsigned ha = wr * 8192 + lane * 16;
  unsigned wa[4];
#pragma unroll
  for (int j = 0; j < 4; ++j) wa[j] = B_RING + (4 * g + q) * 256 + (((wc * 4 + j) ^ ((4 * g + q) & 7)) << 5) + 8 * p;
  f32x4 acc[4][4];
  bf16x8 hf[2][4], wf[2][4];
  auto seg = [&](int c, int ks, unsigned slot, bool doread, bool first = false) {
    bf16x4 lo[4];
#pragma unroll
    for (int m = 0; m < 16; ++m) {
      if (first) MFMA0(acc[m & 3][m >> 2], wf[c][m >> 2], hf[c][m & 3]);
      else MFMA(acc[m & 3][m >> 2], wf[c][m >> 2], hf[c][m & 3]);
      pin();
      if (doread && m < 12) {
        if (m >= 2 && m < 6) hf[c ^ 1][m - 2] = lds128(slot + ha + (m - 2) * 2048 + ks * 1024);
        else {
          const int jj = m < 2 ? 0 : (m - 4) >> 1;
          if ((m & 1) == 0) lo[jj] = ldstr(slot + wa[jj] + ks * 32 * 256);
          else wf[c ^ 1][jj] = cat(lo[jj], ldstr(slot + wa[jj] + (ks * 32 + 16) * 256));
        }
      }
      pin();
    }
  };
  waitl0();
  waitv<0>();
  STAMP(0, 3);
  bar();
  STAMP(1, 1);
#pragma unroll
  for (int i = 0; i < 4; ++i) hf[0][i] = lds128(ha + i * 2048);
#pragma unroll
  for (int j = 0; j < 4; ++j) wf[0][j] = cat(ldstr(wa[j]), ldstr(wa[j] + 16 * 256));
  pin();
#pragma unroll
  for (int r = 0; r < 6; ++r) {
    if (r == 3) {
      waitl0();
      if (lane < 4) *(LAS unsigned*)(4 * ST + ((lane & 1) * 8 + (lane >> 1)) * 1024 + wid * 4) = 1u;
      pin();
    }
    seg(r & 1, (r + 1) & 1, ((r + 1) >> 1) * ST, true, r == 0);
  }
  STAMP(0, 6);
  bar(); pin();
  STAMP(0, 7);
  for (int it = 0; it < 5; ++it) {
#pragma unroll
    for (int u = 0; u < 5; ++u) {
      seg(0, 1, ((u + 3) % 5) * ST, true);
      if (u == 4 && it == 4) break;
      waitl0();
      bar();
      pin();
      seg(1, 0, ((u + 4) % 5) * ST, true);
    }
  }
  seg(1, 0, 0, false);
  pin(); asm volatile("s_nop 15" ::: "memory"); pin();
  STAMP(1, 2);
  const auto rxc = __builtin_amdgcn_make_buffer_rsrc((void*)(ws + WS_XCH), 0, XCH_BYTES, 0x00020000);
  const unsigned mine = (bid * 4 + wid) * 8192 + lane * 16, theirs = ((bid ^ 4) * 4 + wid) * 8192 + lane * 16;
  f32x4 keep[2][4];
#pragma unroll
  for (int i = 0; i < 2; ++i)
#pragma unroll
    for (int j = 0; j < 4; ++j) {
      const f32x4 send = kh ? acc[i][j] : acc[2 + i][j];
      keep[i][j] = kh ? acc[2 + i][j] : acc[i][j];
      __builtin_amdgcn_raw_buffer_store_b128(__builtin_bit_cast(u32x4, send), rxc, mine + (i * 4 + j) * 1024, 0, 16);
    }
  waitv<0>();
  if (lane == 0) st_agent(ws + WS_XFLAG + (bid * 4 + wid) * 128, token);
  STAMP(1, 3);
  {
    const char* pf = ws + WS_XFLAG + ((bid ^ 4) * 4 + wid) * 128;
    for (unsigned spins = 0; spins < SPIN_MAX; ++spins) {
      if (uni(ld_agent(pf)) == token) break;
      __builtin_amdgcn_s_sleep(1);
    }
    asm volatile("" ::: "memory");
  }
  STAMP(1, 4);
  char* ob = (char*)out + (size_t)(pm * 128 + wr * 64 + kh * 32 + fr) * 2048 + (own * 128 + wc * 64 + 4 * g) * 2;
  u32x4 got[2][4];
#pragma unroll
  for (int i = 0; i < 2; ++i)
#pragma unroll
    for (int j = 0; j < 4; ++j) got[i][j] = __builtin_amdgcn_raw_buffer_load_b128(rxc, theirs + (i * 4 + j) * 1024, 0, 16);
  if (pn == 0 && wid == 0 && lane == 0) st_agent(ws + WS_EPOCH + pm * 128, token);
#pragma unroll
  for (int i = 0; i < 2; ++i)
#pragma unroll
    for (int j = 0; j < 4; ++j) {
      const f32x4 o = __builtin_bit_cast(f32x4, got[i][j]);
      const f32x4 s = kh ? o + keep[i][j] : keep[i][j] + o;
      *(u32x2*)(ob + i * 16 * 2048 + j * 32) = (u32x2){pk(s[0], s[1]), pk(s[2], s[3])};
    }
#endif
}

extern "C" void kernel_launch(void* const* d_in, const int* in_sizes, int n_in, void* d_out, int out_size, void* d_ws, size_t ws_size,
                              hipStream_t stream) {
  (void)in_sizes; (void)n_in; (void)out_size; (void)ws_size;
  (void)hipFuncSetAttribute((const void*)mlp_kernel, hipFuncAttributeMaxDynamicSharedMemorySize, LDS_BYTES);
  mlp_kernel<<<256, 512, LDS_BYTES, stream>>>(d_in[0], d_in[1], d_in[2], (char*)d_ws, d_out);
}
```

```cpp
#define PROBE_AT 13
#define PROBE_K 0
#include <hip/hip_runtime.h>
#include <hip/hip_bf16.h>

#define LAS __attribute__((address_space(3)))
#define GAS __attribute__((address_space(1)))
#define DI __device__ __forceinline__
typedef __bf16 bf16x8 __attribute__((ext_vector_type(8)));
typedef __bf16 bf16x4 __attribute__((ext_vector_type(4)));
typedef __bf16 bf16x2 __attribute__((ext_vector_type(2)));
typedef float f32x4 __attribute__((ext_vector_type(4)));
typedef float f32x2 __attribute__((ext_vector_type(2)));
typedef unsigned u32x4 __attribute__((ext_vector_type(4)));
typedef unsigned u32x2 __attribute__((ext_vector_type(2)));
typedef unsigned long long u64;

constexpr int M_ = 2048, D_ = 1024, F_ = 3584;
constexpr unsigned X_BYTES = M_ * D_ * 2, W_BYTES = D_ * F_ * 2, H_BYTES = M_ * F_ * 2;
constexpr unsigned XCH_BYTES = 256u * 32768u;
constexpr unsigned WS_H = 0, WS_XCH = H_BYTES;
constexpr unsigned WS_XFLAG = WS_XCH + XCH_BYTES;
constexpr unsigned WS_HFLAG = WS_XFLAG + 256 * 4 * 128;
constexpr unsigned WS_EPOCH = WS_HFLAG + 256 * 256;

#define G1ROT (4 * (xcd >> 1) + 8 * (xcd & 1) + 2 * (lm >> 2) + 2 * (ln >> 1))
#define PFW2 9
#define ORDER 1
#define ORDSTART (2 * ln)
#define PFL2A -1
#define PFL2B -1
#define POLLDELAY 0
#define POLLSLEEP 1
#define FLAG1AT 0
#define FLAG2AT 2
constexpr unsigned G1_STAGE_B = 44 * 1024;
constexpr unsigned ST = 16384, B_RING = 5 * ST, LDS_BYTES = 10 * ST;
constexpr unsigned SPIN_MAX = 1u << 20;

#if defined(__HIP_DEVICE_COMPILE__)
template <int N> DI void waitv() { asm volatile("s_waitcnt vmcnt(%0)" ::"n"(N) : "memory"); }
DI void waitl0() { asm volatile("" ::: "memory"); __builtin_amdgcn_s_waitcnt(0xc07f); asm volatile("" ::: "memory"); }
DI void bar() { asm volatile("" ::: "memory"); __builtin_amdgcn_s_barrier(); asm volatile("" ::: "memory"); }
DI void pin() { __builtin_amdgcn_sched_barrier(0); }
DI bf16x8 lds128(unsigned a) { return *(const LAS bf16x8*)a; }
DI bf16x4 ldstr(unsigned a) { return __builtin_amdgcn_ds_read_tr16_b64_v4bf16((LAS bf16x4*)a); }
DI bf16x8 cat(bf16x4 a, bf16x4 b) { return __builtin_shufflevector(a, b, 0, 1, 2, 3, 4, 5, 6, 7); }
DI unsigned pk(float lo, float hi) { f32x2 v = {lo, hi}; return __builtin_bit_cast(unsigned, __builtin_convertvector(v, bf16x2)); }
DI float relu(float v) { return __builtin_elementwise_maximum(v, 0.0f); }
DI u64 ld_agent(const char* p) { return __hip_atomic_load((GAS u64*)p, __ATOMIC_RELAXED, __HIP_MEMORY_SCOPE_AGENT); }
DI void st_agent(char* p, u64 v) { __hip_atomic_store((GAS u64*)p, v, __ATOMIC_RELAXED, __HIP_MEMORY_SCOPE_AGENT); }
DI u64 uni(u64 v) { return ((u64)(unsigned)__builtin_amdgcn_readfirstlane((unsigned)(v >> 32)) << 32) | (unsigned)__builtin_amdgcn_readfirstlane((unsigned)v); }
#define MFMA(c, a, b) asm volatile("v_mfma_f32_16x16x32_bf16 %0, %1, %2, %0" : "+v"(c) : "v"(a), "v"(b))
#define MFMA0(c, a, b) asm volatile("v_mfma_f32_16x16x32_bf16 %0, %1, %2, 0" : "=&v"(c) : "v"(a), "v"(b))
#define DMA(rs, lds, vo, so, aux) __builtin_amdgcn_raw_ptr_buffer_load_lds(rs, (LAS void*)(lds), 16, vo, so, 0, aux)
#define STAMP(k, i) do { if ((k) * 8 + (i) == 0 || (k) * 8 + (i) == PROBE_AT) st_r[(k) * 8 + (i)] = __builtin_amdgcn_s_memrealtime(); } while (0)
#define STAMP_DECL u64 st_r[16] = {}
#define STAMP_FLUSH(mask) do {} while (0)
#endif

__global__ __launch_bounds__(512, 2) void mlp_kernel(const void* __restrict__ x, const void* __restrict__ w1, const void* __restrict__ w2,
                                                     char* __restrict__ ws, void* __restrict__ out) {
#if defined(__HIP_DEVICE_COMPILE__)
  const int wid = __builtin_amdgcn_readfirstlane(threadIdx.x >> 6), lane = threadIdx.x & 63;
  const int bid = blockIdx.x, xcd = bid & 7, idx = bid >> 3;
  const int lm = idx & 7, ln = idx >> 3, pm = (xcd & 1) * 8 + lm, pn = (xcd >> 1) * 4 + ln, kh = pn >> 3, own = pn & 7;
  STAMP_DECL;
  STAMP(0, 0);
  const u64 epoch = ld_agent(ws + WS_EPOCH + pm * 128);
  if (wid >= 4) {
    const int lw = wid - 4;
    const auto rx = __builtin_amdgcn_make_buffer_rsrc((void*)x, 0, X_BYTES, 0x00020000);
    const auto rw1 = __builtin_amdgcn_make_buffer_rsrc((void*)w1, 0, W_BYTES, 0x00020000);
    const auto rw2 = __builtin_amdgcn_make_buffer_rsrc((void*)w2, 0, W_BYTES, 0x00020000);
    const auto rh = __builtin_amdgcn_make_buffer_rsrc((void*)(ws + WS_H), 0, H_BYTES, 0x00020000);
    u64 token;
    {
      const int rot = G1ROT;
      unsigned va[4], vb[7];
#pragma unroll
      for (int i = 0; i < 4; ++i) { const int row = (lw * 4 + i) * 8 + (lane >> 3); va[i] = (pm * 128 + row) * 2048 + (((lane & 7) ^ ((row >> 1) & 7)) << 4); }
#pragma unroll
      for (int i = 0; i < 7; ++i) { const int b = (lw * 7 + i) * 1024 + lane * 16, row = b / 448, cb = b - row * 448; vb[i] = row * 7168 + pn * 448 + (cb ^ (((row >> 3) & 1) << 5)); }
      auto issue = [&](int t, unsigned slot) {
        const unsigned kt = (t + rot) & 15, sa = kt * 128, sb = kt * (64 * 7168);
#pragma unroll
        for (int i = 0; i < 4; ++i) DMA(rx, slot + (lw * 4 + i) * 1024, va[i], sa, 0);
#pragma unroll
        for (int i = 0; i < 7; ++i) DMA(rw1, slot + 16384 + (lw * 7 + i) * 1024, vb[i], sb, 0);
      };
      issue(0, 0);
      issue(1, G1_STAGE_B);
      token = uni(epoch) * 6364136223846793005ull + 1442695040888963407ull;
      asm volatile("" ::"s"(token));
      unsigned slot = 2 * G1_STAGE_B;
      waitv<11>();
      bar();
      for (int t = 0; t < 14; ++t) {
        if (t == PFW2) {
          const unsigned o = lane < 56 ? ((bid * 4 + lw) * 56 + lane) * 128 : 0xfffffff0u;
          __builtin_amdgcn_raw_ptr_buffer_load_lds(rw2, (LAS void*)(3 * G1_STAGE_B + lw * 256), 4, o, 0, 0, 0);
        }
        if (t == PFL2A || t == PFL2B) {
          const unsigned o = lane < 56 ? (kh * 1792 + idx * 56 + lw * 14 + (t == PFL2B) * 7 + (lane >> 3)) * 2048 + (own >> 2) * 1024 + (lane & 7) * 128 : 0xfffffff0u;
          __builtin_amdgcn_raw_ptr_buffer_load_lds(rw2, (LAS void*)(3 * G1_STAGE_B + lw * 256), 4, o, 0, 0, 0);
        }
        issue(t + 2, slot);
        slot = slot == 2 * G1_STAGE_B ? 0 : slot + G1_STAGE_B;
        waitv<11>();
        bar();
      }
      waitv<0>();
      bar();
    }
    unsigned va[4], vb[4];
#pragma unroll
    for (int i = 0; i < 4; ++i) va[i] = ((pm * 8 + (lw & 1) * 4 + i) * 112 + kh * 56) * 1024 + lane * 16;
#pragma unroll
    for (int i = 0; i < 4; ++i) { const int row = i * 4 + (lane >> 4); vb[i] = (kh * 1792 + row) * 2048 + own * 256 + (((lane & 15) << 4) ^ ((row & 7) << 5)); }
    unsigned tab;
    {
      const int s = 2 * lane + (lw >> 1), k = s < 21 ? (s - 7) >> 1 : s < 35 ? (s - 21) >> 1 : ((s - 35) * 86) >> 8;
      const int pr = s < 7 ? own : (own ^ (k + 1)) & 7;
      const int r = s < 7 ? s : s < 21 ? (s - 7) & 1 : s < 35 ? 2 + ((s - 21) & 1) : 4 + (s - 35) - 3 * k;
      const int cb = r < 6 ? 7 * (r & 1) + (r & ~1) + (lw & 1) : 6 + 7 * (lw & 1);
      tab = (pr * 7 + r) | (pr * 14 + cb) << 8 | pr * 8 << 16 | (s < 7 ? 0u : r < 2 ? 0x01u : r < 4 ? 0x50u : 0xa0u) << 24;
    }
    auto entry = [&](int T) { return (unsigned)__builtin_amdgcn_readlane(tab, T); };
    auto issueA = [&](int T, unsigned slot) {
      const unsigned so = (entry(T) & 0xff) * 1024;
#pragma unroll
      for (int i = 0; i < 4; ++i) DMA(rh, slot + (((lw & 1) * 4 + i) * 2 + (lw >> 1)) * 1024, va[i], so, 16);
    };
    auto issueB = [&](int T, unsigned slot) {
      const unsigned so = ((entry(T) >> 8) & 0xff) * (16 * 2048);
#pragma unroll
      for (int i = 0; i < 4; ++i) DMA(rw2, B_RING + slot + (lw * 4 + i) * 1024, vb[i], so, 0);
    };
    const char* fp = ws + WS_HFLAG + (pm * 16 + kh * 8 + (lane >> 3)) * 256 + ((lane & 4) ? 128 + (lw & 1) * 32 + (lane & 3) * 8 : 0);
    char* const myflag = ws + WS_HFLAG + (pm * 16 + pn) * 256;
    u64 ready = 0;
    auto needof = [&](int T) { const unsigned e = entry(T); return (u64)(e >> 24) << ((e >> 16) & 0xff); };
    auto ensure = [&](u64 need, bool all) {
      if (__builtin_expect((ready & need) != need, 0)) {
        for (unsigned spins = 0;; ++spins) {
          bool hit = false;
          if ((all && spins == 0) || (((need & ~ready) >> lane) & 1)) hit = ld_agent(fp) == token;
          ready |= __builtin_amdgcn_ballot_w64(hit);
          if (spins >= SPIN_MAX) ready = ~0ull;
          if ((ready & need) == need) break;
          __builtin_amdgcn_s_sleep(POLLSLEEP);
        }
      }
      asm volatile("" ::: "memory");
    };
    auto issue = [&](int T, unsigned slot) { ensure(needof(T), true); issueA(T, slot); issueB(T, slot); };
#pragma unroll
    for (int T = 0; T < 5; ++T) issueB(T, T * ST);
    const unsigned pw = 4 * ST + ((lw & 1) * 8 + (lw >> 1)) * 1024;
    if (lane == 0) *(LAS u32x4*)pw = (u32x4){0u, 0u, 0u, 0u};
    bar();
    waitl0();
    waitv<4>();
    bar();
    if (lw == 0 && lane == 0) st_agent(myflag, token);
    u32x4 hv[5][2];
#pragma unroll
    for (int r = 2; r < 7; ++r)
#pragma unroll
      for (int e = 0; e < 2; ++e) hv[r - 2][e] = *(const LAS u32x4*)((r >> 1) * ST + ((lw * 2 + e) * 2 + (r & 1)) * 1024 + lane * 16);
    const unsigned hb = ((pm * 8 + lw * 2) * 112 + pn * 7) * 1024 + lane * 16;
    auto publish = [&](int r) {
#pragma unroll
      for (int e = 0; e < 2; ++e) __builtin_amdgcn_raw_buffer_store_b128(hv[r - 2][e], rh, hb + (e * 112 + r) * 1024, 0, 16);
    };
    waitl0();
    ensure(needof(3) | needof(4) | needof(5), false);
    STAMP(0, 4);
    if (lw >= 2) issueA(3, 3 * ST);
    for (;;) {
      const u32x4 v = *(volatile const LAS u32x4*)pw;
      if (__builtin_amdgcn_readfirstlane(v[0] & v[1] & v[2] & v[3])) break;
    }
    asm volatile("" ::: "memory");
    STAMP(0, 5);
    issueA(4, 4 * ST);
    issueA(5, 0);
    issueB(5, 0);
    issueB(6, ST);
    publish(2); publish(3);
    waitv<20>();
    bar();
    ensure(needof(6) | needof(7), true);
    issueA(6, ST);
    issueA(7, 2 * ST); issueB(7, 2 * ST);
    waitv<28>();
    bar();
    issue(8, 3 * ST);
    waitv<20>();
    if (lane == 0) st_agent(myflag + 128 + lw * 16, token);
    bar();
    publish(4);
    issue(9, 4 * ST);
    waitv<26>();
    bar();
    publish(5);
    waitv<20>();
    bar();
    publish(6);
    issue(10, 0);
    issue(11, ST);
    waitv<30>();
    bar();
    issue(12, 2 * ST);
    waitv<28>();
    bar();
    issue(13, 3 * ST);
    waitv<24>();
    if (lane == 0) st_agent(myflag + 128 + lw * 16 + 8, token);
    bar();
    unsigned slot = 4 * ST;
    for (int T = 14; T < 28; ++T) {
      issue(T, slot);
      slot = slot == 4 * ST ? 0 : slot + ST;
      waitv<24>();
      bar();
    }
    waitv<16>();
    bar();
    waitv<8>();
    bar();
    waitv<0>();
    bar();
    if (lw == 2) STAMP_FLUSH(0x0030);
    return;
  }
  __builtin_amdgcn_s_setprio(2);
  const int wr = wid >> 1, wc = wid & 1, fr = lane & 15, g = lane >> 4, q = fr >> 2, p = lane & 3;
  const u64 token = uni(epoch) * 6364136223846793005ull + 1442695040888963407ull;
  asm volatile("" ::"s"(token));
  const auto rh = __builtin_amdgcn_make_buffer_rsrc((void*)(ws + WS_H), 0, H_BYTES, 0x00020000);
  const unsigned hg = ((pm * 8 + wr * 4) * 112 + pn * 7) * 1024 + lane * 16;
  {
    unsigned xa[2], wa[2];
#pragma unroll
    for (int ks = 0; ks < 2; ++ks) xa[ks] = (wr * 64 + fr) * 128 + (((4 * ks + g) ^ (fr >> 1)) << 4);
#pragma unroll
    for (int e = 0; e < 2; ++e) wa[e] = 16384 + (8 * g + q) * 448 + wc * 224 + 8 * p + ((((wc + e) & 1) == 0) ? 32 * (g & 1) : -32 * (g & 1));
    f32x4 acc[4][7];
#pragma unroll
    for (int i = 0; i < 4; ++i)
#pragma unroll
      for (int j = 0; j < 7; ++j) { acc[i][j] = (f32x4){0.f, 0.f, 0.f, 0.f}; asm volatile("" : "+v"(acc[i][j])); }
    bf16x8 xf[2][4], wf[2][7];
    auto seg = [&](int c, int ks, unsigned slot, bool doread) {
      bf16x4 lo[7];
#pragma unroll
      for (int m = 0; m < 28; ++m) {
        MFMA(acc[m & 3][m >> 2], wf[c][m >> 2], xf[c][m & 3]);
        pin();
        if (doread && m < 18) {
          if (m >= 2 && m < 6) xf[c ^ 1][m - 2] = lds128(slot + xa[ks] + (m - 2) * 2048);
          else {
            const int jj = m < 2 ? 0 : (m - 4) >> 1;
            if ((m & 1) == 0) lo[jj] = ldstr(slot + wa[jj & 1] + jj * 32 + ks * 32 * 448);
            else wf[c ^ 1][jj] = cat(lo[jj], ldstr(slot + wa[jj & 1] + jj * 32 + (ks * 32 + 4) * 448));
          }
        }
        pin();
      }
    };
    bar();
    STAMP(0, 1);
#pragma unroll
    for (int i = 0; i < 4; ++i) xf[0][i] = lds128(xa[0] + i * 2048);
#pragma unroll
    for (int j = 0; j < 7; ++j) wf[0][j] = cat(ldstr(wa[j & 1] + j * 32), ldstr(wa[j & 1] + j * 32 + 4 * 448));
    for (int t = 0; t < 5; ++t) {
#pragma unroll
      for (int u = 0; u < 3; ++u) {
        seg(0, 1, u * G1_STAGE_B, true);
        waitl0();
        bar();
        pin();
        seg(1, 0, ((u + 1) % 3) * G1_STAGE_B, true);
      }
    }
    seg(0, 1, 0, true);
    waitl0();
    bar();
    pin();
    STAMP(0, 2);
    const unsigned hl = wr * 8192 + wc * 1024 + lane * 16;
    u32x2 hp[4][7];
    auto fin = [&](int n) {
      const int i = n & 3, j = n >> 2;
      const f32x4 v = acc[i][j];
      hp[i][j] = (u32x2){pk(relu(v[0]), relu(v[1])), pk(relu(v[2]), relu(v[3]))};
      if (j == 6) *(LAS u32x2*)(wr * 8192 + lane * 16 + wc * 8 + 3 * ST + i * 2048) = hp[i][6];
      else if (j & 1) {
        const u32x4 w = {hp[i][j - 1][0], hp[i][j - 1][1], hp[i][j][0], hp[i][j][1]};
        if (j == 1) __builtin_amdgcn_raw_buffer_store_b128(w, rh, hg + wc * 1024 + i * 112 * 1024, 0, 16);
        *(LAS u32x4*)(hl + (j >> 1) * ST + i * 2048) = w;
      }
    };
#pragma unroll
    for (int m = 0; m < 28; ++m) {
      MFMA(acc[m & 3][m >> 2], wf[1][m >> 2], xf[1][m & 3]);
      pin();
      if (m >= 3) fin(m - 3);
      pin();
    }
    asm volatile("s_nop 7" ::: "memory");
    pin();
    fin(25); fin(26); fin(27);
  }
  const unsigned ha = wr * 8192 + lane * 16;
  unsigned wa[4];
#pragma unroll
  for (int j = 0; j < 4; ++j) wa[j] = B_RING + (4 * g + q) * 256 + (((wc * 4 + j) ^ ((4 * g + q) & 7)) << 5) + 8 * p;
  f32x4 acc[4][4];
  bf16x8 hf[2][4], wf[2][4];
  auto seg = [&](int c, int ks, unsigned slot, bool doread, bool first = false) {
    bf16x4 lo[4];
#pragma unroll
    for (int m = 0; m < 16; ++m) {
      if (first) MFMA0(acc[m & 3][m >> 2], wf[c][m >> 2], hf[c][m & 3]);
      else MFMA(acc[m & 3][m >> 2], wf[c][m >> 2], hf[c][m & 3]);
      pin();
      if (doread && m < 12) {
        if (m >= 2 && m < 6) hf[c ^ 1][m - 2] = lds128(slot + ha + (m - 2) * 2048 + ks * 1024);
        else {
          const int jj = m < 2 ? 0 : (m - 4) >> 1;
          if ((m & 1) == 0) lo[jj] = ldstr(slot + wa[jj] + ks * 32 * 256);
          else wf[c ^ 1][jj] = cat(lo[jj], ldstr(slot + wa[jj] + (ks * 32 + 16) * 256));
        }
      }
      pin();
    }
  };
  waitl0();
  waitv<0>();
  STAMP(0, 3);
  bar();
  STAMP(1, 1);
#pragma unroll
  for (int i = 0; i < 4; ++i) hf[0][i] = lds128(ha + i * 2048);
#pragma unroll
  for (int j = 0; j < 4; ++j) wf[0][j] = cat(ldstr(wa[j]), ldstr(wa[j] + 16 * 256));
  pin();
#pragma unroll
  for (int r = 0; r < 6; ++r) {
    if (r == 3) {
      waitl0();
      if (lane < 4) *(LAS unsigned*)(4 * ST + ((lane & 1) * 8 + (lane >> 1)) * 1024 + wid * 4) = 1u;
      pin();
    }
    seg(r & 1, (r + 1) & 1, ((r + 1) >> 1) * ST, true, r == 0);
  }
  STAMP(0, 6);
  bar(); pin();
  STAMP(0, 7);
  for (int it = 0; it < 5; ++it) {
#pragma unroll
    for (int u = 0; u < 5; ++u) {
      seg(0, 1, ((u + 3) % 5) * ST, true);
      if (u == 4 && it == 4) break;
      waitl0();
      bar();
      if (u == 4 && it == 0) STAMP(1, 6);
      if (u == 4 && it == 2) STAMP(1, 7);
      pin();
      seg(1, 0, ((u + 4) % 5) * ST, true);
    }
  }
  seg(1, 0, 0, false);
  pin(); asm volatile("s_nop 15" ::: "memory"); pin();
  STAMP(1, 2);
  const auto rxc = __builtin_amdgcn_make_buffer_rsrc((void*)(ws + WS_XCH), 0, XCH_BYTES, 0x00020000);
  const unsigned mine = (bid * 4 + wid) * 8192 + lane * 16, theirs = ((bid ^ 4) * 4 + wid) * 8192 + lane * 16;
  f32x4 keep[2][4];
#pragma unroll
  for (int i = 0; i < 2; ++i)
#pragma unroll
    for (int j = 0; j < 4; ++j) {
      const f32x4 send = kh ? acc[i][j] : acc[2 + i][j];
      keep[i][j] = kh ? acc[2 + i][j] : acc[i][j];
      __builtin_amdgcn_raw_buffer_store_b128(__builtin_bit_cast(u32x4, send), rxc, mine + (i * 4 + j) * 1024, 0, 16);
    }
  waitv<0>();
  if (lane == 0) st_agent(ws + WS_XFLAG + (bid * 4 + wid) * 128, token);
  STAMP(1, 3);
  {
    const char* pf = ws + WS_XFLAG + ((bid ^ 4) * 4 + wid) * 128;
    for (unsigned spins = 0; spins < SPIN_MAX; ++spins) {
      if (uni(ld_agent(pf)) == token) break;
      __builtin_amdgcn_s_sleep(1);
    }
    asm volatile("" ::: "memory");
  }
  STAMP(1, 4);
  char* ob = (char*)out + (size_t)(pm * 128 + wr * 64 + kh * 32 + fr) * 2048 + (own * 128 + wc * 64 + 4 * g) * 2;
  u32x4 got[2][4];
#pragma unroll
  for (int i = 0; i < 2; ++i)
#pragma unroll
    for (int j = 0; j < 4; ++j) got[i][j] = __builtin_amdgcn_raw_buffer_load_b128(rxc, theirs + (i * 4 + j) * 1024, 0, 16);
  if (pn == 0 && wid == 0 && lane == 0) st_agent(ws + WS_EPOCH + pm * 128, token);
#pragma unroll
  for (int i = 0; i < 2; ++i)
#pragma unroll
    for (int j = 0; j < 4; ++j) {
      const f32x4 o = __builtin_bit_cast(f32x4, got[i][j]);
      const f32x4 s = kh ? o + keep[i][j] : keep[i][j] + o;
      *(u32x2*)(ob + i * 16 * 2048 + j * 32) = (u32x2){pk(s[0], s[1]), pk(s[2], s[3])};
    }
  waitv<0>();
  STAMP(1, 5);
  { const u64 lim = st_r[0] + 6000 + PROBE_K * (st_r[PROBE_AT] - st_r[0]);
    while (__builtin_amdgcn_s_memrealtime() < lim) __builtin_amdgcn_s_sleep(1); }
#endif
}

extern "C" void kernel_launch(void* const* d_in, const int* in_sizes, int n_in, void* d_out, int out_size, void* d_ws, size_t ws_size,
                              hipStream_t stream) {
  (void)in_sizes; (void)n_in; (void)out_size; (void)ws_size;
  (void)hipFuncSetAttribute((const void*)mlp_kernel, hipFuncAttributeMaxDynamicSharedMemorySize, LDS_BYTES);
  mlp_kernel<<<256, 512, LDS_BYTES, stream>>>(d_in[0], d_in[1], d_in[2], (char*)d_ws, d_out);
}
```

```cpp
#define PROBE_AT 13
#define PROBE_K 0
#include <hip/hip_runtime.h>
#include <hip/hip_bf16.h>

#define LAS __attribute__((address_space(3)))
#define GAS __attribute__((address_space(1)))
#define DI __device__ __forceinline__
typedef __bf16 bf16x8 __attribute__((ext_vector_type(8)));
typedef __bf16 bf16x4 __attribute__((ext_vector_type(4)));
typedef __bf16 bf16x2 __attribute__((ext_vector_type(2)));
typedef float f32x4 __attribute__((ext_vector_type(4)));
typedef float f32x2 __attribute__((ext_vector_type(2)));
typedef unsigned u32x4 __attribute__((ext_vector_type(4)));
typedef unsigned u32x2 __attribute__((ext_vector_type(2)));
typedef unsigned long long u64;

constexpr int M_ = 2048, D_ = 1024, F_ = 3584;
constexpr unsigned X_BYTES = M_ * D_ * 2, W_BYTES = D_ * F_ * 2, H_BYTES = M_ * F_ * 2;
constexpr unsigned XCH_BYTES = 256u * 32768u;
constexpr unsigned WS_H = 0, WS_XCH = H_BYTES;
constexpr unsigned WS_XFLAG = WS_XCH + XCH_BYTES;
constexpr unsigned WS_HFLAG = WS_XFLAG + 256 * 4 * 128;
constexpr unsigned WS_EPOCH = WS_HFLAG + 256 * 256;

#define G1ROT (4 * (xcd >> 1) + 8 * (xcd & 1) + 2 * (lm >> 2) + 2 * (ln >> 1))
#define PFW2 9
#define ORDER 1
#define ORDSTART (2 * ln)
#define PFL2A -1
#define PFL2B -1
#define POLLDELAY 0
#define POLLSLEEP 1
#define FLAG1AT 0
#define FLAG2AT 2
constexpr unsigned G1_STAGE_B = 44 * 1024;
constexpr unsigned ST = 16384, B_RING = 5 * ST, LDS_BYTES = 10 * ST;
constexpr unsigned SPIN_MAX = 1u << 20;

#if defined(__HIP_DEVICE_COMPILE__)
template <int N> DI void waitv() { asm volatile("s_waitcnt vmcnt(%0)" ::"n"(N) : "memory"); }
DI void waitl0() { asm volatile("" ::: "memory"); __builtin_amdgcn_s_waitcnt(0xc07f); asm volatile("" ::: "memory"); }
DI void bar() { asm volatile("" ::: "memory"); __builtin_amdgcn_s_barrier(); asm volatile("" ::: "memory"); }
DI void pin() { __builtin_amdgcn_sched_barrier(0); }
DI bf16x8 lds128(unsigned a) { return *(const LAS bf16x8*)a; }
DI bf16x4 ldstr(unsigned a) { return __builtin_amdgcn_ds_read_tr16_b64_v4bf16((LAS bf16x4*)a); }
DI bf16x8 cat(bf16x4 a, bf16x4 b) { return __builtin_shufflevector(a, b, 0, 1, 2, 3, 4, 5, 6, 7); }
DI unsigned pk(float lo, float hi) { f32x2 v = {lo, hi}; return __builtin_bit_cast(unsigned, __builtin_convertvector(v, bf16x2)); }
DI float relu(float v) { return __builtin_elementwise_maximum(v, 0.0f); }
DI u64 ld_agent(const char* p) { return __hip_atomic_load((GAS u64*)p, __ATOMIC_RELAXED, __HIP_MEMORY_SCOPE_AGENT); }
DI void st_agent(char* p, u64 v) { __hip_atomic_store((GAS u64*)p, v, __ATOMIC_RELAXED, __HIP_MEMORY_SCOPE_AGENT); }
DI u64 uni(u64 v) { return ((u64)(unsigned)__builtin_amdgcn_readfirstlane((unsigned)(v >> 32)) << 32) | (unsigned)__builtin_amdgcn_readfirstlane((unsigned)v); }
#define MFMA(c, a, b) asm volatile("v_mfma_f32_16x16x32_bf16 %0, %1, %2, %0" : "+v"(c) : "v"(a), "v"(b))
#define MFMA0(c, a, b) asm volatile("v_mfma_f32_16x16x32_bf16 %0, %1, %2, 0" : "=&v"(c) : "v"(a), "v"(b))
#define DMA(rs, lds, vo, so, aux) __builtin_amdgcn_raw_ptr_buffer_load_lds(rs, (LAS void*)(lds), 16, vo, so, 0, aux)
#define STAMP(k, i) do { if ((k) * 8 + (i) == 0 || (k) * 8 + (i) == PROBE_AT) st_r[(k) * 8 + (i)] = __builtin_amdgcn_s_memrealtime(); } while (0)
#define STAMP_DECL u64 st_r[16] = {}
#define STAMP_FLUSH(mask) do {} while (0)
#endif

__global__ __launch_bounds__(512, 2) void mlp_kernel(const void* __restrict__ x, const void* __restrict__ w1, const void* __restrict__ w2,
                                                     char* __restrict__ ws, void* __restrict__ out) {
#if defined(__HIP_DEVICE_COMPILE__)
  const int wid = __builtin_amdgcn_readfirstlane(threadIdx.x >> 6), lane = threadIdx.x & 63;
  const int bid = blockIdx.x, xcd = bid & 7, idx = bid >> 3;
  const int lm = idx & 7, ln = idx >> 3, pm = (xcd & 1) * 8 + lm, pn = (xcd >> 1) * 4 + ln, kh = pn >> 3, own = pn & 7;
  STAMP_DECL;
  STAMP(0, 0);
  const u64 epoch = ld_agent(ws + WS_EPOCH + pm * 128);
  if (wid >= 4) {
    const int lw = wid - 4;
    const auto rx = __builtin_amdgcn_make_buffer_rsrc((void*)x, 0, X_BYTES, 0x00020000);
    const auto rw1 = __builtin_amdgcn_make_buffer_rsrc((void*)w1, 0, W_BYTES, 0x00020000);
    const auto rw2 = __builtin_amdgcn_make_buffer_rsrc((void*)w2, 0, W_BYTES, 0x00020000);
    const auto rh = __builtin_amdgcn_make_buffer_rsrc((void*)(ws + WS_H), 0, H_BYTES, 0x00020000);
    u64 token;
    {
      const int rot = G1ROT;
      unsigned va[4], vb[7];
#pragma unroll
      for (int i = 0; i < 4; ++i) { const int row = (lw * 4 + i) * 8 + (lane >> 3); va[i] = (pm * 128 + row) * 2048 + (((lane & 7) ^ ((row >> 1) & 7)) << 4); }
#pragma unroll
      for (int i = 0; i < 7; ++i) { const int b = (lw * 7 + i) * 1024 + lane * 16, row = b / 448, cb = b - row * 448; vb[i] = row * 7168 + pn * 448 + (cb ^ (((row >> 3) & 1) << 5)); }
      auto issue = [&](int t, unsigned slot) {
        const unsigned kt = (t + rot) & 15, sa = kt * 128, sb = kt * (64 * 7168);
#pragma unroll
        for (int i = 0; i < 4; ++i) DMA(rx, slot + (lw * 4 + i) * 1024, va[i], sa, 0);
#pragma unroll
        for (int i = 0; i < 7; ++i) DMA(rw1, slot + 16384 + (lw * 7 + i) * 1024, vb[i], sb, 0);
      };
      issue(0, 0);
      issue(1, G1_STAGE_B);
      token = uni(epoch) * 6364136223846793005ull + 1442695040888963407ull;
      asm volatile("" ::"s"(token));
      unsigned slot = 2 * G1_STAGE_B;
      waitv<11>();
      bar();
      for (int t = 0; t < 14; ++t) {
        if (t == PFW2) {
          const unsigned o = lane < 56 ? ((bid * 4 + lw) * 56 + lane) * 128 : 0xfffffff0u;
          __builtin_amdgcn_raw_ptr_buffer_load_lds(rw2, (LAS void*)(3 * G1_STAGE_B + lw * 256), 4, o, 0, 0, 0);
        }
        if (t == PFL2A || t == PFL2B) {
          const unsigned o = lane < 56 ? (kh * 1792 + idx * 56 + lw * 14 + (t == PFL2B) * 7 + (lane >> 3)) * 2048 + (own >> 2) * 1024 + (lane & 7) * 128 : 0xfffffff0u;
          __builtin_amdgcn_raw_ptr_buffer_load_lds(rw2, (LAS void*)(3 * G1_STAGE_B + lw * 256), 4, o, 0, 0, 0);
        }
        issue(t + 2, slot);
        slot = slot == 2 * G1_STAGE_B ? 0 : slot + G1_STAGE_B;
        waitv<11>();
        bar();
      }
      waitv<0>();
      bar();
    }
    unsigned va[4], vb[4];
#pragma unroll
    for (int i = 0; i < 4; ++i) va[i] = ((pm * 8 + (lw & 1) * 4 + i) * 112 + kh * 56) * 1024 + lane * 16;
#pragma unroll
    for (int i = 0; i < 4; ++i) { const int row = i * 4 + (lane >> 4); vb[i] = (kh * 1792 + row) * 2048 + own * 256 + (((lane & 15) << 4) ^ ((row & 7) << 5)); }
    unsigned tab;
    {
      const int s = 2 * lane + (lw >> 1), k = s < 21 ? (s - 7) >> 1 : s < 35 ? (s - 21) >> 1 : ((s - 35) * 86) >> 8;
      const int pr = s < 7 ? own : (own ^ (k + 1)) & 7;
      const int r = s < 7 ? s : s < 21 ? (s - 7) & 1 : s < 35 ? 2 + ((s - 21) & 1) : 4 + (s - 35) - 3 * k;
      const int cb = r < 6 ? 7 * (r & 1) + (r & ~1) + (lw & 1) : 6 + 7 * (lw & 1);
      tab = (pr * 7 + r) | (pr * 14 + cb) << 8 | pr * 8 << 16 | (s < 7 ? 0u : r < 2 ? 0x01u : r < 4 ? 0x50u : 0xa0u) << 24;
    }
    auto entry = [&](int T) { return (unsigned)__builtin_amdgcn_readlane(tab, T); };
    auto issueA = [&](int T, unsigned slot) {
      const unsigned so = (entry(T) & 0xff) * 1024;
#pragma unroll
      for (int i = 0; i < 4; ++i) DMA(rh, slot + (((lw & 1) * 4 + i) * 2 + (lw >> 1)) * 1024, va[i], so, 16);
    };
    auto issueB = [&](int T, unsigned slot) {
      const unsigned so = ((entry(T) >> 8) & 0xff) * (16 * 2048);
#pragma unroll
      for (int i = 0; i < 4; ++i) DMA(rw2, B_RING + slot + (lw * 4 + i) * 1024, vb[i], so, 0);
    };
    const char* fp = ws + WS_HFLAG + (pm * 16 + kh * 8 + (lane >> 3)) * 256 + ((lane & 4) ? 128 + (lw & 1) * 32 + (lane & 3) * 8 : 0);
    char* const myflag = ws + WS_HFLAG + (pm * 16 + pn) * 256;
    u64 ready = 0;
    auto needof = [&](int T) { const unsigned e = entry(T); return (u64)(e >> 24) << ((e >> 16) & 0xff); };
    auto ensure = [&](u64 need, bool all) {
      if (__builtin_expect((ready & need) != need, 0)) {
        for (unsigned spins = 0;; ++spins) {
          bool hit = false;
          if ((all && spins == 0) || (((need & ~ready) >> lane) & 1)) hit = ld_agent(fp) == token;
          ready |= __builtin_amdgcn_ballot_w64(hit);
          if (spins >= SPIN_MAX) ready = ~0ull;
          if ((ready & need) == need) break;
          __builtin_amdgcn_s_sleep(POLLSLEEP);
        }
      }
      asm volatile("" ::: "memory");
    };
    auto issue = [&](int T, unsigned slot) { ensure(needof(T), true); issueA(T, slot); issueB(T, slot); };
#pragma unroll
    for (int T = 0; T < 5; ++T) issueB(T, T * ST);
    const unsigned pw = 4 * ST + ((lw & 1) * 8 + (lw >> 1)) * 1024;
    if (lane == 0) *(LAS u32x4*)pw = (u32x4){0u, 0u, 0u, 0u};
    bar();
    waitl0();
    waitv<4>();
    bar();
    if (lw == 0 && lane == 0) st_agent(myflag, token);
    u32x4 hv[5][2];
#pragma unroll
    for (int r = 2; r < 7; ++r)
#pragma unroll
      for (int e = 0; e < 2; ++e) hv[r - 2][e] = *(const LAS u32x4*)((r >> 1) * ST + ((lw * 2 + e) * 2 + (r & 1)) * 1024 + lane * 16);
    const unsigned hb = ((pm * 8 + lw * 2) * 112 + pn * 7) * 1024 + lane * 16;
    auto publish = [&](int r) {
#pragma unroll
      for (int e = 0; e < 2; ++e) __builtin_amdgcn_raw_buffer_store_b128(hv[r - 2][e], rh, hb + (e * 112 + r) * 1024, 0, 16);
    };
    waitl0();
    ensure(needof(3) | needof(4) | needof(5), false);
    STAMP(0, 4);
    if (lw >= 2) issueA(3, 3 * ST);
    for (;;) {
      const u32x4 v = *(volatile const LAS u32x4*)pw;
      if (__builtin_amdgcn_readfirstlane(v[0] & v[1] & v[2] & v[3])) break;
    }
    asm volatile("" ::: "memory");
    STAMP(0, 5);
    issueA(4, 4 * ST);
    issueA(5, 0);
    issueB(5, 0);
    issueB(6, ST);
    publish(2); publish(3);
    waitv<20>();
    bar();
    ensure(needof(6) | needof(7), true);
    issueA(6, ST);
    issueA(7, 2 * ST); issueB(7, 2 * ST);
    waitv<28>();
    bar();
    issue(8, 3 * ST);
    waitv<20>();
    if (lane == 0) st_agent(myflag + 128 + lw * 16, token);
    bar();
    publish(4);
    issue(9, 4 * ST);
    waitv<26>();
    bar();
    publish(5);
    waitv<20>();
    bar();
    publish(6);
    issue(10, 0);
    issue(11, ST);
    waitv<30>();
    bar();
    issue(12, 2 * ST);
    waitv<28>();
    bar();
    issue(13, 3 * ST);
    waitv<24>();
    if (lane == 0) st_agent(myflag + 128 + lw * 16 + 8, token);
    bar();
    unsigned slot = 4 * ST;
    for (int T = 14; T < 28; ++T) {
      issue(T, slot);
      slot = slot == 4 * ST ? 0 : slot + ST;
      waitv<24>();
      bar();
    }
    waitv<16>();
    bar();
    waitv<8>();
    bar();
    waitv<0>();
    bar();
    if (lw == 2) STAMP_FLUSH(0x0030);
    return;
  }
  __builtin_amdgcn_s_setprio(2);
  const int wr = wid >> 1, wc = wid & 1, fr = lane & 15, g = lane >> 4, q = fr >> 2, p = lane & 3;
  const u64 token = uni(epoch) * 6364136223846793005ull + 1442695040888963407ull;
  asm volatile("" ::"s"(token));
  const auto rh = __builtin_amdgcn_make_buffer_rsrc((void*)(ws + WS_H), 0, H_BYTES, 0x00020000);
  const unsigned hg = ((pm * 8 + wr * 4) * 112 + pn * 7) * 1024 + lane * 16;
  {
    unsigned xa[2], wa[2];
#pragma unroll
    for (int ks = 0; ks < 2; ++ks) xa[ks] = (wr * 64 + fr) * 128 + (((4 * ks + g) ^ (fr >> 1)) << 4);
#pragma unroll
    for (int e = 0; e < 2; ++e) wa[e] = 16384 + (8 * g + q) * 448 + wc * 224 + 8 * p + ((((wc + e) & 1) == 0) ? 32 * (g & 1) : -32 * (g & 1));
    f32x4 acc[4][7];
#pragma unroll
    for (int i = 0; i < 4; ++i)
#pragma unroll
      for (int j = 0; j < 7; ++j) { acc[i][j] = (f32x4){0.f, 0.f, 0.f, 0.f}; asm volatile("" : "+v"(acc[i][j])); }
    bf16x8 xf[2][4], wf[2][7];
    auto seg = [&](int c, int ks, unsigned slot, bool doread) {
      bf16x4 lo[7];
#pragma unroll
      for (int m = 0; m < 28; ++m) {
        MFMA(acc[m & 3][m >> 2], wf[c][m >> 2], xf[c][m & 3]);
        pin();
        if (doread && m < 18) {
          if (m >= 2 && m < 6) xf[c ^ 1][m - 2] = lds128(slot + xa[ks] + (m - 2) * 2048);
          else {
            const int jj = m < 2 ? 0 : (m - 4) >> 1;
            if ((m & 1) == 0) lo[jj] = ldstr(slot + wa[jj & 1] + jj * 32 + ks * 32 * 448);
            else wf[c ^ 1][jj] = cat(lo[jj], ldstr(slot + wa[jj & 1] + jj * 32 + (ks * 32 + 4) * 448));
          }
        }
        pin();
      }
    };
    bar();
    STAMP(0, 1);
#pragma unroll
    for (int i = 0; i < 4; ++i) xf[0][i] = lds128(xa[0] + i * 2048);
#pragma unroll
    for (int j = 0; j < 7; ++j) wf[0][j] = cat(ldstr(wa[j & 1] + j * 32), ldstr(wa[j & 1] + j * 32 + 4 * 448));
    for (int t = 0; t < 5; ++t) {
#pragma unroll
      for (int u = 0; u < 3; ++u) {
        seg(0, 1, u * G1_STAGE_B, true);
        waitl0();
        bar();
        pin();
        seg(1, 0, ((u + 1) % 3) * G1_STAGE_B, true);
      }
    }
    seg(0, 1, 0, true);
    waitl0();
    bar();
    pin();
    STAMP(0, 2);
    const unsigned hl = wr * 8192 + wc * 1024 + lane * 16;
    u32x2 hp[4][7];
    auto fin = [&](int n) {
      const int i = n & 3, j = n >> 2;
      const f32x4 v = acc[i][j];
      hp[i][j] = (u32x2){pk(relu(v[0]), relu(v[1])), pk(relu(v[2]), relu(v[3]))};
      if (j == 6) *(LAS u32x2*)(wr * 8192 + lane * 16 + wc * 8 + 3 * ST + i * 2048) = hp[i][6];
      else if (j & 1) {
        const u32x4 w = {hp[i][j - 1][0], hp[i][j - 1][1], hp[i][j][0], hp[i][j][1]};
        if (j == 1) __builtin_amdgcn_raw_buffer_store_b128(w, rh, hg + wc * 1024 + i * 112 * 1024, 0, 16);
        *(LAS u32x4*)(hl + (j >> 1) * ST + i * 2048) = w;
      }
    };
#pragma unroll
    for (int m = 0; m < 28; ++m) {
      MFMA(acc[m & 3][m >> 2], wf[1][m >> 2], xf[1][m & 3]);
      pin();
      if (m >= 3) fin(m - 3);
      pin();
    }
    asm volatile("s_nop 7" ::: "memory");
    pin();
    fin(25); fin(26); fin(27);
  }
  const unsigned ha = wr * 8192 + lane * 16;
  unsigned wa[4];
#pragma unroll
  for (int j = 0; j < 4; ++j) wa[j] = B_RING + (4 * g + q) * 256 + (((wc * 4 + j) ^ ((4 * g + q) & 7)) << 5) + 8 * p;
  f32x4 acc[4][4];
  bf16x8 hf[2][4], wf[2][4];
  auto seg = [&](int c, int ks, unsigned slot, bool doread, bool first = false) {
    bf16x4 lo[4];
#pragma unroll
    for (int m = 0; m < 16; ++m) {
      if (first) MFMA0(acc[m & 3][m >> 2], wf[c][m >> 2], hf[c][m & 3]);
      else MFMA(acc[m & 3][m >> 2], wf[c][m >> 2], hf[c][m & 3]);
      pin();
      if (doread && m < 12) {
        if (m >= 2 && m < 6) hf[c ^ 1][m - 2] = lds128(slot + ha + (m - 2) * 2048 + ks * 1024);
        else {
          const int jj = m < 2 ? 0 : (m - 4) >> 1;
          if ((m & 1) == 0) lo[jj] = ldstr(slot + wa[jj] + ks * 32 * 256);
          else wf[c ^ 1][jj] = cat(lo[jj], ldstr(slot + wa[jj] + (ks * 32 + 16) * 256));
        }
      }
      pin();
    }
  };
  waitl0();
  waitv<0>();
  STAMP(0, 3);
  bar();
  STAMP(1, 1);
#pragma unroll
  for (int i = 0; i < 4; ++i) hf[0][i] = lds128(ha + i * 2048);
#pragma unroll
  for (int j = 0; j < 4; ++j) wf[0][j] = cat(ldstr(wa[j]), ldstr(wa[j] + 16 * 256));
  pin();
#pragma unroll
  for (int r = 0; r < 6; ++r) {
    if (r == 3) {
      waitl0();
      if (lane < 4) *(LAS unsigned*)(4 * ST + ((lane & 1) * 8 + (lane >> 1)) * 1024 + wid * 4) = 1u;
      pin();
    }
    seg(r & 1, (r + 1) & 1, ((r + 1) >> 1) * ST, true, r == 0);
  }
  STAMP(0, 6);
  bar(); pin();
  STAMP(0, 7);
  for (int it = 0; it < 5; ++it) {
#pragma unroll
    for (int u = 0; u < 5; ++u) {
      seg(0, 1, ((u + 3) % 5) * ST, true);
      if (u == 4 && it == 4) break;
      waitl0();
      bar();
      if (u == 4 && it == 0) STAMP(1, 6);
      if (u == 4 && it == 2) STAMP(1, 7);
      pin();
      seg(1, 0, ((u + 4) % 5) * ST, true);
    }
  }
  seg(1, 0, 0, false);
  pin(); asm volatile("s_nop 15" ::: "memory"); pin();
  STAMP(1, 2);
  const auto rxc = __builtin_amdgcn_make_buffer_rsrc((void*)(ws + WS_XCH), 0, XCH_BYTES, 0x00020000);
  const unsigned mine = (bid * 4 + wid) * 8192 + lane * 16, theirs = ((bid ^ 4) * 4 + wid) * 8192 + lane * 16;
  f32x4 keep[2][4];
#pragma unroll
  for (int i = 0; i < 2; ++i)
#pragma unroll
    for (int j = 0; j < 4; ++j) {
      const f32x4 send = kh ? acc[i][j] : acc[2 + i][j];
      keep[i][j] = kh ? acc[2 + i][j] : acc[i][j];
      __builtin_amdgcn_raw_buffer_store_b128(__builtin_bit_cast(u32x4, send), rxc, mine + (i * 4 + j) * 1024, 0, 16);
    }
  waitv<0>();
  if (lane == 0) st_agent(ws + WS_XFLAG + (bid * 4 + wid) * 128, token);
  STAMP(1, 3);
  {
    const char* pf = ws + WS_XFLAG + ((bid ^ 4) * 4 + wid) * 128;
    for (unsigned spins = 0; spins < SPIN_MAX; ++spins) {
      if (uni(ld_agent(pf)) == token) break;
      __builtin_amdgcn_s_sleep(1);
    }
    asm volatile("" ::: "memory");
  }
  STAMP(1, 4);
  char* ob = (char*)out + (size_t)(pm * 128 + wr * 64 + kh * 32 + fr) * 2048 + (own * 128 + wc * 64 + 4 * g) * 2;
  u32x4 got[2][4];
#pragma unroll
  for (int i = 0; i < 2; ++i)
#pragma unroll
    for (int j = 0; j < 4; ++j) got[i][j] = __builtin_amdgcn_raw_buffer_load_b128(rxc, theirs + (i * 4 + j) * 1024, 0, 16);
  if (pn == 0 && wid == 0 && lane == 0) st_agent(ws + WS_EPOCH + pm * 128, token);
#pragma unroll
  for (int i = 0; i < 2; ++i)
#pragma unroll
    for (int j = 0; j < 4; ++j) {
      const f32x4 o = __builtin_bit_cast(f32x4, got[i][j]);
      const f32x4 s = kh ? o + keep[i][j] : keep[i][j] + o;
      *(u32x2*)(ob + i * 16 * 2048 + j * 32) = (u32x2){pk(s[0], s[1]), pk(s[2], s[3])};
    }
  waitv<0>();
  STAMP(1, 5);

#endif
}

__global__ void tiny_a(char* ws) {
#if defined(__HIP_DEVICE_COMPILE__)
  const u64 t = __builtin_amdgcn_s_memrealtime();
  if (threadIdx.x == 0) st_agent(ws + WS_EPOCH + 16 * 128, t);
#endif
}
__global__ void tiny_b(char* ws) {
#if defined(__HIP_DEVICE_COMPILE__)
  const u64 now = __builtin_amdgcn_s_memrealtime();
  u64 lim = uni(ld_agent(ws + WS_EPOCH + 16 * 128)) + 6000;
  if (lim > now + 7000) lim = now;
  while (__builtin_amdgcn_s_memrealtime() < lim) __builtin_amdgcn_s_sleep(1);
#endif
}
extern "C" void kernel_launch(void* const* d_in, const int* in_sizes, int n_in, void* d_out, int out_size, void* d_ws, size_t ws_size,
                              hipStream_t stream) {
  (void)in_sizes; (void)n_in; (void)out_size; (void)ws_size;
  (void)hipFuncSetAttribute((const void*)mlp_kernel, hipFuncAttributeMaxDynamicSharedMemorySize, LDS_BYTES);
  tiny_a<<<1, 64, 0, stream>>>((char*)d_ws);
  mlp_kernel<<<256, 512, LDS_BYTES, stream>>>(d_in[0], d_in[1], d_in[2], (char*)d_ws, d_out);
  tiny_b<<<1, 64, 0, stream>>>((char*)d_ws);
}
```

```cpp
#include <hip/hip_runtime.h>
#include <hip/hip_bf16.h>

#define LAS __attribute__((address_space(3)))
#define GAS __attribute__((address_space(1)))
#define DI __device__ __forceinline__
typedef __bf16 bf16x8 __attribute__((ext_vector_type(8)));
typedef __bf16 bf16x4 __attribute__((ext_vector_type(4)));
typedef __bf16 bf16x2 __attribute__((ext_vector_type(2)));
typedef float f32x4 __attribute__((ext_vector_type(4)));
typedef float f32x2 __attribute__((ext_vector_type(2)));
typedef unsigned u32x4 __attribute__((ext_vector_type(4)));
typedef unsigned u32x2 __attribute__((ext_vector_type(2)));
typedef unsigned long long u64;

constexpr int M_ = 2048, D_ = 1024, F_ = 3584;
constexpr unsigned X_BYTES = M_ * D_ * 2, W_BYTES = D_ * F_ * 2, H_BYTES = M_ * F_ * 2;
constexpr unsigned XCH_BYTES = 256u * 32768u;
constexpr unsigned WS_H = 0, WS_XCH = H_BYTES;
constexpr unsigned WS_XFLAG = WS_XCH + XCH_BYTES;
constexpr unsigned WS_HFLAG = WS_XFLAG + 256 * 4 * 128;
constexpr unsigned WS_EPOCH = WS_HFLAG + 256 * 256;

#define G1ROT (4 * (xcd >> 1) + 8 * (xcd & 1) + 2 * (lm >> 2) + 2 * (ln >> 1))
#define PFW2 9
#define ORDER 1
#define ORDSTART (2 * ln)
#define PFL2A -1
#define PFL2B -1
#define POLLDELAY 0
#define POLLSLEEP 1
#define FLAG1AT 0
#define FLAG2AT 2
#define PUBAUX 16
#define TAILR 4
#define LDAT 5
#define CLRAT 2
#define OUTAUX 16
constexpr unsigned G1_STAGE_B = 44 * 1024;
constexpr unsigned ST = 16384, B_RING = 5 * ST, LDS_BYTES = 10 * ST;
constexpr unsigned SPIN_MAX = 1u << 16;

#if defined(__HIP_DEVICE_COMPILE__)
template <int N> DI void waitv() { asm volatile("s_waitcnt vmcnt(%0)" ::"n"(N) : "memory"); }
DI void waitl0() { asm volatile("" ::: "memory"); __builtin_amdgcn_s_waitcnt(0xc07f); asm volatile("" ::: "memory"); }
DI void bar() { asm volatile("" ::: "memory"); __builtin_amdgcn_s_barrier(); asm volatile("" ::: "memory"); }
DI void pin() { __builtin_amdgcn_sched_barrier(0); }
DI bf16x8 lds128(unsigned a) { return *(const LAS bf16x8*)a; }
DI bf16x4 ldstr(unsigned a) { return __builtin_amdgcn_ds_read_tr16_b64_v4bf16((LAS bf16x4*)a); }
DI bf16x8 cat(bf16x4 a, bf16x4 b) { return __builtin_shufflevector(a, b, 0, 1, 2, 3, 4, 5, 6, 7); }
DI unsigned pk(float lo, float hi) { f32x2 v = {lo, hi}; return __builtin_bit_cast(unsigned, __builtin_convertvector(v, bf16x2)); }
DI float relu(float v) { return __builtin_elementwise_maximum(v, 0.0f); }
DI u64 ld_agent(const char* p) { return __hip_atomic_load((GAS u64*)p, __ATOMIC_RELAXED, __HIP_MEMORY_SCOPE_AGENT); }
DI void st_agent(char* p, u64 v) { __hip_atomic_store((GAS u64*)p, v, __ATOMIC_RELAXED, __HIP_MEMORY_SCOPE_AGENT); }
DI u64 uni(u64 v) { return ((u64)(unsigned)__builtin_amdgcn_readfirstlane((unsigned)(v >> 32)) << 32) | (unsigned)__builtin_amdgcn_readfirstlane((unsigned)v); }
#define MFMA(c, a, b) asm volatile("v_mfma_f32_16x16x32_bf16 %0, %1, %2, %0" : "+v"(c) : "v"(a), "v"(b))
#define MFMA0(c, a, b) asm volatile("v_mfma_f32_16x16x32_bf16 %0, %1, %2, 0" : "=&v"(c) : "v"(a), "v"(b))
#define DMA(rs, lds, vo, so, aux) __builtin_amdgcn_raw_ptr_buffer_load_lds(rs, (LAS void*)(lds), 16, vo, so, 0, aux)
#define STAMP(k, i) do {} while (0)
#define STAMP_DECL
#define STAMP_FLUSH(mask) do {} while (0)
#endif

__global__ __launch_bounds__(512, 2) void mlp_kernel(const void* __restrict__ x, const void* __restrict__ w1, const void* __restrict__ w2,
                                                     char* __restrict__ ws, void* __restrict__ out) {
#if defined(__HIP_DEVICE_COMPILE__)
  const int wid = __builtin_amdgcn_readfirstlane(threadIdx.x >> 6), lane = threadIdx.x & 63;
  const int bid = blockIdx.x, xcd = bid & 7, idx = bid >> 3;
  const int lm = idx & 7, ln = idx >> 3, pm = (xcd & 1) * 8 + lm, pn = (xcd >> 1) * 4 + ln, kh = pn >> 3, own = pn & 7;
  STAMP_DECL;
  STAMP(0, 0);
  const u64 epoch = ld_agent(ws + WS_EPOCH + pm * 128);
  if (wid >= 4) {
    const int lw = wid - 4;
    const auto rx = __builtin_amdgcn_make_buffer_rsrc((void*)x, 0, X_BYTES, 0x00020000);
    const auto rw1 = __builtin_amdgcn_make_buffer_rsrc((void*)w1, 0, W_BYTES, 0x00020000);
    const auto rw2 = __builtin_amdgcn_make_buffer_rsrc((void*)w2, 0, W_BYTES, 0x00020000);
    const auto rh = __builtin_amdgcn_make_buffer_rsrc((void*)(ws + WS_H), 0, H_BYTES, 0x00020000);
    u64 token;
    {
      const int rot = G1ROT;
      unsigned va[4], vb[7];
#pragma unroll
      for (int i = 0; i < 4; ++i) { const int row = (lw * 4 + i) * 8 + (lane >> 3); va[i] = (pm * 128 + row) * 2048 + (((lane & 7) ^ ((row >> 1) & 7)) << 4); }
#pragma unroll
      for (int i = 0; i < 7; ++i) { const int b = (lw * 7 + i) * 1024 + lane * 16, row = b / 448, cb = b - row * 448; vb[i] = row * 7168 + pn * 448 + (cb ^ (((row >> 3) & 1) << 5)); }
      auto issue = [&](int t, unsigned slot) {
        const unsigned kt = (t + rot) & 15, sa = kt * 128, sb = kt * (64 * 7168);
#pragma unroll
        for (int i = 0; i < 4; ++i) DMA(rx, slot + (lw * 4 + i) * 1024, va[i], sa, 0);
#pragma unroll
        for (int i = 0; i < 7; ++i) DMA(rw1, slot + 16384 + (lw * 7 + i) * 1024, vb[i], sb, 0);
      };
      issue(0, 0);
      issue(1, G1_STAGE_B);
      token = uni(epoch) + 0x9e3779b97f4a7c15ull;
      asm volatile("" ::"s"(token));
      unsigned slot = 2 * G1_STAGE_B;
      waitv<11>();
      bar();
      for (int t = 0; t < 14; ++t) {
        if (t == PFW2) {
          const unsigned o = lane < 56 ? ((bid * 4 + lw) * 56 + lane) * 128 : 0xfffffff0u;
          __builtin_amdgcn_raw_ptr_buffer_load_lds(rw2, (LAS void*)(3 * G1_STAGE_B + lw * 256), 4, o, 0, 0, 0);
        }
        if (t == PFL2A || t == PFL2B) {
          const unsigned o = lane < 56 ? (kh * 1792 + idx * 56 + lw * 14 + (t == PFL2B) * 7 + (lane >> 3)) * 2048 + (own >> 2) * 1024 + (lane & 7) * 128 : 0xfffffff0u;
          __builtin_amdgcn_raw_ptr_buffer_load_lds(rw2, (LAS void*)(3 * G1_STAGE_B + lw * 256), 4, o, 0, 0, 0);
        }
        issue(t + 2, slot);
        slot = slot == 2 * G1_STAGE_B ? 0 : slot + G1_STAGE_B;
        waitv<11>();
        bar();
      }
      waitv<0>();
      bar();
    }
    unsigned va[4], vb[4];
#pragma unroll
    for (int i = 0; i < 4; ++i) va[i] = ((pm * 8 + (lw & 1) * 4 + i) * 112 + kh * 56) * 1024 + lane * 16;
#pragma unroll
    for (int i = 0; i < 4; ++i) { const int row = i * 4 + (lane >> 4); vb[i] = (kh * 1792 + row) * 2048 + own * 256 + (((lane & 15) << 4) ^ ((row & 7) << 5)); }
    unsigned tab;
    {
      const int s = 2 * lane + (lw >> 1), k = s < 21 ? (s - 7) >> 1 : s < 35 ? (s - 21) >> 1 : ((s - 35) * 86) >> 8;
      const int pr = s < 7 ? own : (own ^ (k + 1)) & 7;
      const int r = s < 7 ? s : s < 21 ? (s - 7) & 1 : s < 35 ? 2 + ((s - 21) & 1) : 4 + (s - 35) - 3 * k;
      const int cb = r < 6 ? 7 * (r & 1) + (r & ~1) + (lw & 1) : 6 + 7 * (lw & 1);
      tab = (pr * 7 + r) | (pr * 14 + cb) << 8 | pr * 8 << 16 | (s < 7 ? 0u : r < 2 ? 0x01u : r < 4 ? 0x50u : 0xa0u) << 24;
    }
    auto entry = [&](int T) { return (unsigned)__builtin_amdgcn_readlane(tab, T); };
    auto issueA = [&](int T, unsigned slot) {
      const unsigned so = (entry(T) & 0xff) * 1024;
#pragma unroll
      for (int i = 0; i < 4; ++i) DMA(rh, slot + (((lw & 1) * 4 + i) * 2 + (lw >> 1)) * 1024, va[i], so, 16);
    };
    auto issueB = [&](int T, unsigned slot) {
      const unsigned so = ((entry(T) >> 8) & 0xff) * (16 * 2048);
#pragma unroll
      for (int i = 0; i < 4; ++i) DMA(rw2, B_RING + slot + (lw * 4 + i) * 1024, vb[i], so, 0);
    };
    const char* fp = ws + WS_HFLAG + (pm * 16 + kh * 8 + (lane >> 3)) * 256 + ((lane & 4) ? 128 + (lw & 1) * 32 + (lane & 3) * 8 : 0);
    char* const myflag = ws + WS_HFLAG + (pm * 16 + pn) * 256;
    u64 ready = 0;
    auto needof = [&](int T) { const unsigned e = entry(T); return (u64)(e >> 24) << ((e >> 16) & 0xff); };
    auto ensure = [&](u64 need, bool all) {
      if (__builtin_expect((ready & need) != need, 0)) {
        for (unsigned spins = 0;; ++spins) {
          bool hit = false;
          if ((all && spins == 0) || (((need & ~ready) >> lane) & 1)) hit = ld_agent(fp) == token;
          ready |= __builtin_amdgcn_ballot_w64(hit);
          if (spins >= SPIN_MAX) ready = ~0ull;
          if ((ready & need) == need) break;
          __builtin_amdgcn_s_sleep(POLLSLEEP);
        }
      }
      asm volatile("" ::: "memory");
    };
    auto issue = [&](int T, unsigned slot) { ensure(needof(T), true); issueA(T, slot); issueB(T, slot); };
#pragma unroll
    for (int T = 0; T < 5; ++T) issueB(T, T * ST);
    const unsigned pw = 4 * ST + ((lw & 1) * 8 + (lw >> 1)) * 1024;
    if (lane == 0) *(LAS u32x4*)pw = (u32x4){0u, 0u, 0u, 0u};
    bar();
    waitl0();
    waitv<4>();
    bar();
    if (lw == 0 && lane == 0) st_agent(myflag, token);
    u32x4 hv[5][2];
#pragma unroll
    for (int r = 2; r < 7; ++r)
#pragma unroll
      for (int e = 0; e < 2; ++e) hv[r - 2][e] = *(const LAS u32x4*)((r >> 1) * ST + ((lw * 2 + e) * 2 + (r & 1)) * 1024 + lane * 16);
    const unsigned hb = ((pm * 8 + lw * 2) * 112 + pn * 7) * 1024 + lane * 16;
    auto publish = [&](int r) {
#pragma unroll
      for (int e = 0; e < 2; ++e)
        __builtin_amdgcn_raw_buffer_store_b128(hv[r - 2][e], rh, hb + (e * 112 + r) * 1024, 0, PUBAUX);
    };
    waitl0();
    ensure(needof(3) | needof(4) | needof(5), false);
    STAMP(0, 4);
    if (lw >= 2) issueA(3, 3 * ST);
    for (;;) {
      const u32x4 v = *(volatile const LAS u32x4*)pw;
      if (__builtin_amdgcn_readfirstlane(v[0] & v[1] & v[2] & v[3])) break;
    }
    asm volatile("" ::: "memory");
    STAMP(0, 5);
    issueA(4, 4 * ST);
    issueA(5, 0);
    issueB(5, 0);
    issueB(6, ST);
    publish(2); publish(3);
    waitv<20>();
    bar();
    ensure(needof(6) | needof(7), true);
    issueA(6, ST);
    issueA(7, 2 * ST); issueB(7, 2 * ST);
    waitv<28>();
    bar();
    issue(8, 3 * ST);
    waitv<20>();
    if (lane == 0) st_agent(myflag + 128 + lw * 16, token);
    bar();
    publish(4);
    issue(9, 4 * ST);
    waitv<26>();
    bar();
    publish(5);
    waitv<20>();
    bar();
    publish(6);
    issue(10, 0);
    issue(11, ST);
    waitv<30>();
    bar();
    issue(12, 2 * ST);
    waitv<28>();
    bar();
    issue(13, 3 * ST);
    waitv<24>();
    if (lane == 0) st_agent(myflag + 128 + lw * 16 + 8, token);
    bar();
    unsigned slot = 4 * ST;
    for (int T = 14; T < 28; ++T) {
      issue(T, slot);
      slot = slot == 4 * ST ? 0 : slot + ST;
      waitv<24>();
      bar();
    }
    waitv<16>();
    bar();
    waitv<8>();
    bar();
    waitv<0>();
    bar();
    if (lw == 2) STAMP_FLUSH(0x0030);
    return;
  }
  __builtin_amdgcn_s_setprio(2);
  const int wr = wid >> 1, wc = wid & 1, fr = lane & 15, g = lane >> 4, q = fr >> 2, p = lane & 3;
  const u64 token = uni(epoch) + 0x9e3779b97f4a7c15ull;
  asm volatile("" ::"s"(token));
  const auto rh = __builtin_amdgcn_make_buffer_rsrc((void*)(ws + WS_H), 0, H_BYTES, 0x00020000);
  const unsigned hg = ((pm * 8 + wr * 4) * 112 + pn * 7) * 1024 + lane * 16;
  const auto rxc = __builtin_amdgcn_make_buffer_rsrc((void*)(ws + WS_XCH), 0, XCH_BYTES, 0x00020000);
  const unsigned mine = (bid * 4 + wid) * 8192 + lane * 16, theirs = ((bid ^ 4) * 4 + wid) * 8192 + lane * 16;
  {
    unsigned xa[2], wa[2];
#pragma unroll
    for (int ks = 0; ks < 2; ++ks) xa[ks] = (wr * 64 + fr) * 128 + (((4 * ks + g) ^ (fr >> 1)) << 4);
#pragma unroll
    for (int e = 0; e < 2; ++e) wa[e] = 16384 + (8 * g + q) * 448 + wc * 224 + 8 * p + ((((wc + e) & 1) == 0) ? 32 * (g & 1) : -32 * (g & 1));
    f32x4 acc[4][7];
#pragma unroll
    for (int i = 0; i < 4; ++i)
#pragma unroll
      for (int j = 0; j < 7; ++j) { acc[i][j] = (f32x4){0.f, 0.f, 0.f, 0.f}; asm volatile("" : "+v"(acc[i][j])); }
    bf16x8 xf[2][4], wf[2][7];
    auto seg = [&](int c, int ks, unsigned slot, bool doread) {
      bf16x4 lo[7];
#pragma unroll
      for (int m = 0; m < 28; ++m) {
        MFMA(acc[m & 3][m >> 2], wf[c][m >> 2], xf[c][m & 3]);
        pin();
        if (doread && m < 18) {
          if (m >= 2 && m < 6) xf[c ^ 1][m - 2] = lds128(slot + xa[ks] + (m - 2) * 2048);
          else {
            const int jj = m < 2 ? 0 : (m - 4) >> 1;
            if ((m & 1) == 0) lo[jj] = ldstr(slot + wa[jj & 1] + jj * 32 + ks * 32 * 448);
            else wf[c ^ 1][jj] = cat(lo[jj], ldstr(slot + wa[jj & 1] + jj * 32 + (ks * 32 + 4) * 448));
          }
        }
        pin();
      }
    };
    const u32x4 ones = {~0u, ~0u, ~0u, ~0u};
    bar();
    STAMP(0, 1);
#pragma unroll
    for (int i = 0; i < 4; ++i) xf[0][i] = lds128(xa[0] + i * 2048);
#pragma unroll
    for (int j = 0; j < 7; ++j) wf[0][j] = cat(ldstr(wa[j & 1] + j * 32), ldstr(wa[j & 1] + j * 32 + 4 * 448));
    for (int t = 0; t < 5; ++t) {
#pragma unroll
      for (int u = 0; u < 3; ++u) {
        seg(0, 1, u * G1_STAGE_B, true);
        waitl0();
        bar();
        pin();
        seg(1, 0, ((u + 1) % 3) * G1_STAGE_B, true);
      }
    }
    seg(0, 1, 0, true);
    waitl0();
    bar();
    pin();
    STAMP(0, 2);
    const unsigned hl = wr * 8192 + wc * 1024 + lane * 16;
    u32x2 hp[4][7];
    auto fin = [&](int n) {
      const int i = n & 3, j = n >> 2;
      const f32x4 v = acc[i][j];
      hp[i][j] = (u32x2){pk(relu(v[0]), relu(v[1])), pk(relu(v[2]), relu(v[3]))};
      if (j == 6) *(LAS u32x2*)(wr * 8192 + lane * 16 + wc * 8 + 3 * ST + i * 2048) = hp[i][6];
      else if (j & 1) {
        const u32x4 w = {hp[i][j - 1][0], hp[i][j - 1][1], hp[i][j][0], hp[i][j][1]};
        if (j == 1) __builtin_amdgcn_raw_buffer_store_b128(w, rh, hg + wc * 1024 + i * 112 * 1024, 0, 16);
        *(LAS u32x4*)(hl + (j >> 1) * ST + i * 2048) = w;
      }
    };
#pragma unroll
    for (int m = 0; m < 28; ++m) {
      MFMA(acc[m & 3][m >> 2], wf[1][m >> 2], xf[1][m & 3]);
      pin();
      if (m >= 3) fin(m - 3);
      pin();
    }
    asm volatile("s_nop 7" ::: "memory");
    pin();
    fin(25); fin(26); fin(27);
  }
  const unsigned ha[2] = {wr * 8192u + (kh ^ 1) * 4096u + lane * 16u, wr * 8192u + kh * 4096u + lane * 16u};
  unsigned wa[4];
#pragma unroll
  for (int j = 0; j < 4; ++j) wa[j] = B_RING + (4 * g + q) * 256 + (((wc * 4 + j) ^ ((4 * g + q) & 7)) << 5) + 8 * p;
  f32x4 acc[4][4];
  bf16x8 hf[2][4], wf[2][4];
  auto seg = [&](int c, int ks, unsigned slot, bool doread, bool first = false) {
    bf16x4 lo[4];
#pragma unroll
    for (int m = 0; m < 16; ++m) {
      if (first) MFMA0(acc[m & 3][m >> 2], wf[c][m >> 2], hf[c][m & 3]);
      else MFMA(acc[m & 3][m >> 2], wf[c][m >> 2], hf[c][m & 3]);
      pin();
      if (doread && m < 12) {
        if (m >= 2 && m < 6) hf[c ^ 1][m - 2] = lds128(slot + ha[(m - 2) >> 1] + (m & 1) * 2048 + ks * 1024);
        else {
          const int jj = m < 2 ? 0 : (m - 4) >> 1;
          if ((m & 1) == 0) lo[jj] = ldstr(slot + wa[jj] + ks * 32 * 256);
          else wf[c ^ 1][jj] = cat(lo[jj], ldstr(slot + wa[jj] + (ks * 32 + 16) * 256));
        }
      }
      pin();
    }
  };
  waitl0();
  waitv<0>();
  STAMP(0, 3);
  bar();
  STAMP(1, 1);
#pragma unroll
  for (int i = 0; i < 4; ++i) hf[0][i] = lds128(ha[i >> 1] + (i & 1) * 2048);
#pragma unroll
  for (int j = 0; j < 4; ++j) wf[0][j] = cat(ldstr(wa[j]), ldstr(wa[j] + 16 * 256));
  pin();
#pragma unroll
  for (int r = 0; r < 6; ++r) {
    if (r == 3) {
      waitl0();
      if (lane < 4) *(LAS unsigned*)(4 * ST + ((lane & 1) * 8 + (lane >> 1)) * 1024 + wid * 4) = 1u;
      pin();
    }
    seg(r & 1, (r + 1) & 1, ((r + 1) >> 1) * ST, true, r == 0);
  }
  STAMP(0, 6);
  bar(); pin();
  STAMP(0, 7);
  seg(0, 1, 3 * ST, true);
  waitl0();
  bar();
  pin();
  auto tile = [&](int u) {
    seg(1, 0, (u % 5) * ST, true);
    seg(0, 1, (u % 5) * ST, true);
    waitl0();
    bar();
    pin();
  };
#pragma unroll
  for (int u = 0; u < (24 - TAILR) % 5; ++u) tile(4 + u);
  for (int it = 0; it < (24 - TAILR) / 5; ++it) {
#pragma unroll
    for (int u = 0; u < 5; ++u) tile(4 + (24 - TAILR) % 5 + u);
  }
  bf16x8 ta[2][4], tb[2][8];
  bf16x4 tl[8];
  u32x4 got[8];
  auto rd = [&](int c, int half, int T, int k) {
    const unsigned slot = (T % 5) * ST;
    const int ks = k / 10, r = k % 10;
    if (r == 2 || r == 3) ta[c][ks * 2 + r - 2] = lds128(slot + ha[half] + (r - 2) * 2048 + ks * 1024);
    else {
      const int jj = r < 2 ? 0 : (r - 2) >> 1;
      if ((r & 1) == 0) tl[ks * 4 + jj] = ldstr(slot + wa[jj] + ks * 32 * 256);
      else tb[c][ks * 4 + jj] = cat(tl[ks * 4 + jj], ldstr(slot + wa[jj] + (ks * 32 + 16) * 256));
    }
  };
  auto fetch = [&](int n) { got[n] = __builtin_amdgcn_raw_buffer_load_b128(rxc, mine + n * 1024, 0, 16); };
  pin();
#pragma unroll
  for (int m = 0; m < 16; ++m) {
    MFMA(acc[m & 3][m >> 2], wf[1][m >> 2], hf[1][m & 3]);
    pin();
    if (m < 10) { rd(0, 0, 28 - TAILR, 2 * m); rd(0, 0, 28 - TAILR, 2 * m + 1); }
    pin();
  }
#pragma unroll
  for (int n = 0; n < 2 * TAILR; ++n) {
    const int c = n & 1, half = n >= TAILR;
    if (n < TAILR - 1) { bar(); pin(); }
    if (n == TAILR) {
      STAMP(1, 3);
      asm volatile("" ::: "memory");
      pin();
    }
#pragma unroll
    for (int m = 0; m < 16; ++m) {
      MFMA(acc[2 * half + (m & 1)][(m >> 1) & 3], tb[c][m >> 1], ta[c][(m >> 3) * 2 + (m & 1)]);
      pin();
      if (n + 1 < 2 * TAILR && m < 10) {
        const int n1 = n + 1, h1 = n1 >= TAILR, T1 = 28 - TAILR + n1 - h1 * TAILR;
        rd(c ^ 1, h1, T1, 2 * m); rd(c ^ 1, h1, T1, 2 * m + 1);
      }
      if (n == TAILR && m >= 3 && m < 11) {
        const int e = m - 3;
        u32x4 v = __builtin_bit_cast(u32x4, acc[e & 1][e >> 1]);
#pragma unroll
        for (int z = 0; z < 4; ++z) v[z] = (v[z] & ~0xffu) | ((unsigned)(token >> (8 * z)) & 0xffu);
        __builtin_amdgcn_raw_buffer_store_b128(v, rxc, theirs + e * 1024, 0, 16);
      }
      if (n == TAILR + LDAT && m >= 8) fetch(m - 8);
      pin();
    }
  }
  pin(); asm volatile("s_nop 15" ::: "memory"); pin();
  STAMP(1, 2);
  if (LDAT >= TAILR) {
#pragma unroll
    for (int n = 0; n < 8; ++n) fetch(n);
  }
  for (unsigned spins = 0; spins < SPIN_MAX; ++spins) {
    unsigned bad = 0;
#pragma unroll
    for (int z = 0; z < 4; ++z) {
      unsigned a = got[0][z], o = a;
#pragma unroll
      for (int n = 1; n < 8; ++n) { a &= got[n][z]; o |= got[n][z]; }
      const unsigned t = (unsigned)(token >> (8 * z));
      bad |= (a ^ t) | (o ^ t);
    }
    if (__builtin_amdgcn_ballot_w64((bad & 0xffu) != 0) == 0) break;
    asm volatile("" ::: "memory");
#pragma unroll
    for (int n = 0; n < 8; ++n) fetch(n);
  }
  STAMP(1, 4);
  if (pn == 0 && wid == 0 && lane == 0) st_agent(ws + WS_EPOCH + pm * 128, token);
  const auto ro = __builtin_amdgcn_make_buffer_rsrc(out, 0, X_BYTES, 0x00020000);
  const unsigned ob = (pm * 128 + wr * 64 + kh * 32 + fr) * 2048 + (own * 128 + wc * 64 + (g & 1) * 16 + (g >> 1) * 8) * 2;
#pragma unroll
  for (int i = 0; i < 2; ++i)
#pragma unroll
    for (int jp = 0; jp < 2; ++jp) {
      unsigned w[2][2];
#pragma unroll
      for (int e = 0; e < 2; ++e) {
        const f32x4 s = acc[2 + i][2 * jp + e] + __builtin_bit_cast(f32x4, got[(2 * jp + e) * 2 + i] & ~0xffu);
        w[e][0] = pk(s[0], s[1]); w[e][1] = pk(s[2], s[3]);
      }
      const auto s0 = __builtin_amdgcn_permlane16_swap(w[0][0], w[1][0], false, false);
      const auto s1 = __builtin_amdgcn_permlane16_swap(w[0][1], w[1][1], false, false);
      __builtin_amdgcn_raw_buffer_store_b128((u32x4){s0[0], s1[0], s0[1], s1[1]}, ro, ob + i * 16 * 2048 + jp * 64, 0, OUTAUX);
    }
#endif
}

extern "C" void kernel_launch(void* const* d_in, const int* in_sizes, int n_in, void* d_out, int out_size, void* d_ws, size_t ws_size,
                              hipStream_t stream) {
  (void)in_sizes; (void)n_in; (void)out_size; (void)ws_size;
  (void)hipFuncSetAttribute((const void*)mlp_kernel, hipFuncAttributeMaxDynamicSharedMemorySize, LDS_BYTES);
  mlp_kernel<<<256, 512, LDS_BYTES, stream>>>(d_in[0], d_in[1], d_in[2], (char*)d_ws, d_out);
}
```

```cpp
#define PROBE_AT 13
#define PROBE_K 0
#include <hip/hip_runtime.h>
#include <hip/hip_bf16.h>

#define LAS __attribute__((address_space(3)))
#define GAS __attribute__((address_space(1)))
#define DI __device__ __forceinline__
typedef __bf16 bf16x8 __attribute__((ext_vector_type(8)));
typedef __bf16 bf16x4 __attribute__((ext_vector_type(4)));
typedef __bf16 bf16x2 __attribute__((ext_vector_type(2)));
typedef float f32x4 __attribute__((ext_vector_type(4)));
typedef float f32x2 __attribute__((ext_vector_type(2)));
typedef unsigned u32x4 __attribute__((ext_vector_type(4)));
typedef unsigned u32x2 __attribute__((ext_vector_type(2)));
typedef unsigned long long u64;

constexpr int M_ = 2048, D_ = 1024, F_ = 3584;
constexpr unsigned X_BYTES = M_ * D_ * 2, W_BYTES = D_ * F_ * 2, H_BYTES = M_ * F_ * 2;
constexpr unsigned XCH_BYTES = 256u * 32768u;
constexpr unsigned WS_H = 0, WS_XCH = H_BYTES;
constexpr unsigned WS_XFLAG = WS_XCH + XCH_BYTES;
constexpr unsigned WS_HFLAG = WS_XFLAG + 256 * 4 * 128;
constexpr unsigned WS_EPOCH = WS_HFLAG + 256 * 256;

#define G1ROT (4 * (xcd >> 1) + 8 * (xcd & 1) + 2 * (lm >> 2) + 2 * (ln >> 1))
#define PFW2 9
#define ORDER 1
#define ORDSTART (2 * ln)
#define PFL2A -1
#define PFL2B -1
#define POLLDELAY 0
#define POLLSLEEP 1
#define FLAG1AT 0
#define FLAG2AT 2
#define PUBAUX 16
#define TAILR 5
#define LDAT 5
#define CLRAT 2
#define OUTAUX 16
constexpr unsigned G1_STAGE_B = 44 * 1024;
constexpr unsigned ST = 16384, B_RING = 5 * ST, LDS_BYTES = 10 * ST;
constexpr unsigned SPIN_MAX = 1u << 20;

#if defined(__HIP_DEVICE_COMPILE__)
template <int N> DI void waitv() { asm volatile("s_waitcnt vmcnt(%0)" ::"n"(N) : "memory"); }
DI void waitl0() { asm volatile("" ::: "memory"); __builtin_amdgcn_s_waitcnt(0xc07f); asm volatile("" ::: "memory"); }
DI void bar() { asm volatile("" ::: "memory"); __builtin_amdgcn_s_barrier(); asm volatile("" ::: "memory"); }
DI void pin() { __builtin_amdgcn_sched_barrier(0); }
DI bf16x8 lds128(unsigned a) { return *(const LAS bf16x8*)a; }
DI bf16x4 ldstr(unsigned a) { return __builtin_amdgcn_ds_read_tr16_b64_v4bf16((LAS bf16x4*)a); }
DI bf16x8 cat(bf16x4 a, bf16x4 b) { return __builtin_shufflevector(a, b, 0, 1, 2, 3, 4, 5, 6, 7); }
DI unsigned pk(float lo, float hi) { f32x2 v = {lo, hi}; return __builtin_bit_cast(unsigned, __builtin_convertvector(v, bf16x2)); }
DI float relu(float v) { return __builtin_elementwise_maximum(v, 0.0f); }
DI u64 ld_agent(const char* p) { return __hip_atomic_load((GAS u64*)p, __ATOMIC_RELAXED, __HIP_MEMORY_SCOPE_AGENT); }
DI void st_agent(char* p, u64 v) { __hip_atomic_store((GAS u64*)p, v, __ATOMIC_RELAXED, __HIP_MEMORY_SCOPE_AGENT); }
DI u64 uni(u64 v) { return ((u64)(unsigned)__builtin_amdgcn_readfirstlane((unsigned)(v >> 32)) << 32) | (unsigned)__builtin_amdgcn_readfirstlane((unsigned)v); }
#define MFMA(c, a, b) asm volatile("v_mfma_f32_16x16x32_bf16 %0, %1, %2, %0" : "+v"(c) : "v"(a), "v"(b))
#define MFMA0(c, a, b) asm volatile("v_mfma_f32_16x16x32_bf16 %0, %1, %2, 0" : "=&v"(c) : "v"(a), "v"(b))
#define DMA(rs, lds, vo, so, aux) __builtin_amdgcn_raw_ptr_buffer_load_lds(rs, (LAS void*)(lds), 16, vo, so, 0, aux)
#define STAMP(k, i) do { if ((k) * 8 + (i) == PROBE_AT) st_r[(k) * 8 + (i)] = __builtin_amdgcn_s_memrealtime(); } while (0)
#define STAMP_DECL u64 st_r[16] = {}
#define STAMP_FLUSH(mask) do {} while (0)
#endif

__global__ __launch_bounds__(512, 2) void mlp_kernel(const void* __restrict__ x, const void* __restrict__ w1, const void* __restrict__ w2,
                                                     char* __restrict__ ws, void* __restrict__ out) {
#if defined(__HIP_DEVICE_COMPILE__)
  u64 tz; asm volatile("s_memrealtime %0\n s_waitcnt lgkmcnt(0)" : "=s"(tz) :: "memory"); pin();
  const int wid = __builtin_amdgcn_readfirstlane(threadIdx.x >> 6), lane = threadIdx.x & 63;
  const int bid = blockIdx.x, xcd = bid & 7, idx = bid >> 3;
  const int lm = idx & 7, ln = idx >> 3, pm = (xcd & 1) * 8 + lm, pn = (xcd >> 1) * 4 + ln, kh = pn >> 3, own = pn & 7;
  STAMP_DECL;
  STAMP(0, 0);
  const u64 epoch = ld_agent(ws + WS_EPOCH + pm * 128);
  if (wid >= 4) {
    const int lw = wid - 4;
    const auto rx = __builtin_amdgcn_make_buffer_rsrc((void*)x, 0, X_BYTES, 0x00020000);
    const auto rw1 = __builtin_amdgcn_make_buffer_rsrc((void*)w1, 0, W_BYTES, 0x00020000);
    const auto rw2 = __builtin_amdgcn_make_buffer_rsrc((void*)w2, 0, W_BYTES, 0x00020000);
    const auto rh = __builtin_amdgcn_make_buffer_rsrc((void*)(ws + WS_H), 0, H_BYTES, 0x00020000);
    u64 token;
    asm volatile("" :: "s"(x), "s"(w1) : "memory");
    STAMP(0, 4);
    pin();
    {
      const int rot = G1ROT;
      unsigned va[4], vb[7];
#pragma unroll
      for (int i = 0; i < 4; ++i) { const int row = (lw * 4 + i) * 8 + (lane >> 3); va[i] = (pm * 128 + row) * 2048 + (((lane & 7) ^ ((row >> 1) & 7)) << 4); }
#pragma unroll
      for (int i = 0; i < 7; ++i) { const int b = (lw * 7 + i) * 1024 + lane * 16, row = b / 448, cb = b - row * 448; vb[i] = row * 7168 + pn * 448 + (cb ^ (((row >> 3) & 1) << 5)); }
      auto issue = [&](int t, unsigned slot) {
        const unsigned kt = (t + rot) & 15, sa = kt * 128, sb = kt * (64 * 7168);
#pragma unroll
        for (int i = 0; i < 4; ++i) DMA(rx, slot + (lw * 4 + i) * 1024, va[i], sa, 0);
#pragma unroll
        for (int i = 0; i < 7; ++i) DMA(rw1, slot + 16384 + (lw * 7 + i) * 1024, vb[i], sb, 0);
      };
      issue(0, 0);
      pin(); STAMP(0, 5); pin();
      issue(1, G1_STAGE_B);
      token = uni(epoch) + 0x9e3779b97f4a7c15ull;
      asm volatile("" ::"s"(token));
      unsigned slot = 2 * G1_STAGE_B;
      waitv<11>();
      bar();
      for (int t = 0; t < 14; ++t) {
        if (t == PFW2) {
          const unsigned o = lane < 56 ? ((bid * 4 + lw) * 56 + lane) * 128 : 0xfffffff0u;
          __builtin_amdgcn_raw_ptr_buffer_load_lds(rw2, (LAS void*)(3 * G1_STAGE_B + lw * 256), 4, o, 0, 0, 0);
        }
        if (t == PFL2A || t == PFL2B) {
          const unsigned o = lane < 56 ? (kh * 1792 + idx * 56 + lw * 14 + (t == PFL2B) * 7 + (lane >> 3)) * 2048 + (own >> 2) * 1024 + (lane & 7) * 128 : 0xfffffff0u;
          __builtin_amdgcn_raw_ptr_buffer_load_lds(rw2, (LAS void*)(3 * G1_STAGE_B + lw * 256), 4, o, 0, 0, 0);
        }
        issue(t + 2, slot);
        slot = slot == 2 * G1_STAGE_B ? 0 : slot + G1_STAGE_B;
        waitv<11>();
        bar();
      }
      waitv<0>();
      bar();
    }
    unsigned va[4], vb[4];
#pragma unroll
    for (int i = 0; i < 4; ++i) va[i] = ((pm * 8 + (lw & 1) * 4 + i) * 112 + kh * 56) * 1024 + lane * 16;
#pragma unroll
    for (int i = 0; i < 4; ++i) { const int row = i * 4 + (lane >> 4); vb[i] = (kh * 1792 + row) * 2048 + own * 256 + (((lane & 15) << 4) ^ ((row & 7) << 5)); }
    unsigned tab;
    {
      const int s = 2 * lane + (lw >> 1), k = s < 21 ? (s - 7) >> 1 : s < 35 ? (s - 21) >> 1 : ((s - 35) * 86) >> 8;
      const int pr = s < 7 ? own : (own ^ (k + 1)) & 7;
      const int r = s < 7 ? s : s < 21 ? (s - 7) & 1 : s < 35 ? 2 + ((s - 21) & 1) : 4 + (s - 35) - 3 * k;
      const int cb = r < 6 ? 7 * (r & 1) + (r & ~1) + (lw & 1) : 6 + 7 * (lw & 1);
      tab = (pr * 7 + r) | (pr * 14 + cb) << 8 | pr * 8 << 16 | (s < 7 ? 0u : r < 2 ? 0x01u : r < 4 ? 0x50u : 0xa0u) << 24;
    }
    auto entry = [&](int T) { return (unsigned)__builtin_amdgcn_readlane(tab, T); };
    auto issueA = [&](int T, unsigned slot) {
      const unsigned so = (entry(T) & 0xff) * 1024;
#pragma unroll
      for (int i = 0; i < 4; ++i) DMA(rh, slot + (((lw & 1) * 4 + i) * 2 + (lw >> 1)) * 1024, va[i], so, 16);
    };
    auto issueB = [&](int T, unsigned slot) {
      const unsigned so = ((entry(T) >> 8) & 0xff) * (16 * 2048);
#pragma unroll
      for (int i = 0; i < 4; ++i) DMA(rw2, B_RING + slot + (lw * 4 + i) * 1024, vb[i], so, 0);
    };
    const char* fp = ws + WS_HFLAG + (pm * 16 + kh * 8 + (lane >> 3)) * 256 + ((lane & 4) ? 128 + (lw & 1) * 32 + (lane & 3) * 8 : 0);
    char* const myflag = ws + WS_HFLAG + (pm * 16 + pn) * 256;
    u64 ready = 0;
    auto needof = [&](int T) { const unsigned e = entry(T); return (u64)(e >> 24) << ((e >> 16) & 0xff); };
    auto ensure = [&](u64 need, bool all) {
      if (__builtin_expect((ready & need) != need, 0)) {
        for (unsigned spins = 0;; ++spins) {
          bool hit = false;
          if ((all && spins == 0) || (((need & ~ready) >> lane) & 1)) hit = ld_agent(fp) == token;
          ready |= __builtin_amdgcn_ballot_w64(hit);
          if (spins >= SPIN_MAX) ready = ~0ull;
          if ((ready & need) == need) break;
          __builtin_amdgcn_s_sleep(POLLSLEEP);
        }
      }
      asm volatile("" ::: "memory");
    };
    auto issue = [&](int T, unsigned slot) { ensure(needof(T), true); issueA(T, slot); issueB(T, slot); };
#pragma unroll
    for (int T = 0; T < 5; ++T) issueB(T, T * ST);
    const unsigned pw = 4 * ST + ((lw & 1) * 8 + (lw >> 1)) * 1024;
    if (lane == 0) *(LAS u32x4*)pw = (u32x4){0u, 0u, 0u, 0u};
    bar();
    waitl0();
    waitv<4>();
    bar();
    if (lw == 0 && lane == 0) st_agent(myflag, token);
    u32x4 hv[5][2];
#pragma unroll
    for (int r = 2; r < 7; ++r)
#pragma unroll
      for (int e = 0; e < 2; ++e) hv[r - 2][e] = *(const LAS u32x4*)((r >> 1) * ST + ((lw * 2 + e) * 2 + (r & 1)) * 1024 + lane * 16);
    const unsigned hb = ((pm * 8 + lw * 2) * 112 + pn * 7) * 1024 + lane * 16;
    auto publish = [&](int r) {
#pragma unroll
      for (int e = 0; e < 2; ++e)
        __builtin_amdgcn_raw_buffer_store_b128(hv[r - 2][e], rh, hb + (e * 112 + r) * 1024, 0, PUBAUX);
    };
    waitl0();
    ensure(needof(3) | needof(4) | needof(5), false);
    if (lw >= 2) issueA(3, 3 * ST);
    for (;;) {
      const u32x4 v = *(volatile const LAS u32x4*)pw;
      if (__builtin_amdgcn_readfirstlane(v[0] & v[1] & v[2] & v[3])) break;
    }
    asm volatile("" ::: "memory");
    issueA(4, 4 * ST);
    issueA(5, 0);
    issueB(5, 0);
    issueB(6, ST);
    publish(2); publish(3);
    waitv<20>();
    bar();
    ensure(needof(6) | needof(7), true);
    issueA(6, ST);
    issueA(7, 2 * ST); issueB(7, 2 * ST);
    waitv<28>();
    bar();
    issue(8, 3 * ST);
    waitv<20>();
    if (lane == 0) st_agent(myflag + 128 + lw * 16, token);
    bar();
    publish(4);
    issue(9, 4 * ST);
    waitv<26>();
    bar();
    publish(5);
    waitv<20>();
    bar();
    publish(6);
    issue(10, 0);
    issue(11, ST);
    waitv<30>();
    bar();
    issue(12, 2 * ST);
    waitv<28>();
    bar();
    issue(13, 3 * ST);
    waitv<24>();
    if (lane == 0) st_agent(myflag + 128 + lw * 16 + 8, token);
    bar();
    unsigned slot = 4 * ST;
    for (int T = 14; T < 28; ++T) {
      issue(T, slot);
      slot = slot == 4 * ST ? 0 : slot + ST;
      waitv<24>();
      bar();
    }
    waitv<16>();
    bar();
    waitv<8>();
    bar();
    waitv<0>();
    bar();
    if (PROBE_AT == 4 || PROBE_AT == 5) { const u64 lim = tz + 6000 + PROBE_K * (st_r[PROBE_AT] - tz);
    while (__builtin_amdgcn_s_memrealtime() < lim) __builtin_amdgcn_s_sleep(1); }
    return;
  }
  __builtin_amdgcn_s_setprio(2);
  const int wr = wid >> 1, wc = wid & 1, fr = lane & 15, g = lane >> 4, q = fr >> 2, p = lane & 3;
  const u64 token = uni(epoch) + 0x9e3779b97f4a7c15ull;
  asm volatile("" ::"s"(token));
  const auto rh = __builtin_amdgcn_make_buffer_rsrc((void*)(ws + WS_H), 0, H_BYTES, 0x00020000);
  const unsigned hg = ((pm * 8 + wr * 4) * 112 + pn * 7) * 1024 + lane * 16;
  const auto rxc = __builtin_amdgcn_make_buffer_rsrc((void*)(ws + WS_XCH), 0, XCH_BYTES, 0x00020000);
  const unsigned mine = (bid * 4 + wid) * 8192 + lane * 16, theirs = ((bid ^ 4) * 4 + wid) * 8192 + lane * 16;
  {
    unsigned xa[2], wa[2];
#pragma unroll
    for (int ks = 0; ks < 2; ++ks) xa[ks] = (wr * 64 + fr) * 128 + (((4 * ks + g) ^ (fr >> 1)) << 4);
#pragma unroll
    for (int e = 0; e < 2; ++e) wa[e] = 16384 + (8 * g + q) * 448 + wc * 224 + 8 * p + ((((wc + e) & 1) == 0) ? 32 * (g & 1) : -32 * (g & 1));
    f32x4 acc[4][7];
#pragma unroll
    for (int i = 0; i < 4; ++i)
#pragma unroll
      for (int j = 0; j < 7; ++j) { acc[i][j] = (f32x4){0.f, 0.f, 0.f, 0.f}; asm volatile("" : "+v"(acc[i][j])); }
    bf16x8 xf[2][4], wf[2][7];
    auto seg = [&](int c, int ks, unsigned slot, bool doread) {
      bf16x4 lo[7];
#pragma unroll
      for (int m = 0; m < 28; ++m) {
        MFMA(acc[m & 3][m >> 2], wf[c][m >> 2], xf[c][m & 3]);
        pin();
        if (doread && m < 18) {
          if (m >= 2 && m < 6) xf[c ^ 1][m - 2] = lds128(slot + xa[ks] + (m - 2) * 2048);
          else {
            const int jj = m < 2 ? 0 : (m - 4) >> 1;
            if ((m & 1) == 0) lo[jj] = ldstr(slot + wa[jj & 1] + jj * 32 + ks * 32 * 448);
            else wf[c ^ 1][jj] = cat(lo[jj], ldstr(slot + wa[jj & 1] + jj * 32 + (ks * 32 + 4) * 448));
          }
        }
        pin();
      }
    };
    const u32x4 ones = {~0u, ~0u, ~0u, ~0u};
    bar();
    STAMP(0, 1);
#pragma unroll
    for (int i = 0; i < 4; ++i) xf[0][i] = lds128(xa[0] + i * 2048);
#pragma unroll
    for (int j = 0; j < 7; ++j) wf[0][j] = cat(ldstr(wa[j & 1] + j * 32), ldstr(wa[j & 1] + j * 32 + 4 * 448));
    for (int t = 0; t < 5; ++t) {
#pragma unroll
      for (int u = 0; u < 3; ++u) {
        seg(0, 1, u * G1_STAGE_B, true);
        waitl0();
        bar();
        pin();
        seg(1, 0, ((u + 1) % 3) * G1_STAGE_B, true);
      }
    }
    seg(0, 1, 0, true);
    waitl0();
    bar();
    pin();
    STAMP(0, 2);
    const unsigned hl = wr * 8192 + wc * 1024 + lane * 16;
    u32x2 hp[4][7];
    auto fin = [&](int n) {
      const int i = n & 3, j = n >> 2;
      const f32x4 v = acc[i][j];
      hp[i][j] = (u32x2){pk(relu(v[0]), relu(v[1])), pk(relu(v[2]), relu(v[3]))};
      if (j == 6) *(LAS u32x2*)(wr * 8192 + lane * 16 + wc * 8 + 3 * ST + i * 2048) = hp[i][6];
      else if (j & 1) {
        const u32x4 w = {hp[i][j - 1][0], hp[i][j - 1][1], hp[i][j][0], hp[i][j][1]};
        if (j == 1) __builtin_amdgcn_raw_buffer_store_b128(w, rh, hg + wc * 1024 + i * 112 * 1024, 0, 16);
        *(LAS u32x4*)(hl + (j >> 1) * ST + i * 2048) = w;
      }
    };
#pragma unroll
    for (int m = 0; m < 28; ++m) {
      MFMA(acc[m & 3][m >> 2], wf[1][m >> 2], xf[1][m & 3]);
      pin();
      if (m >= 3) fin(m - 3);
      pin();
    }
    asm volatile("s_nop 7" ::: "memory");
    pin();
    fin(25); fin(26); fin(27);
  }
  const unsigned ha[2] = {wr * 8192u + (kh ^ 1) * 4096u + lane * 16u, wr * 8192u + kh * 4096u + lane * 16u};
  unsigned wa[4];
#pragma unroll
  for (int j = 0; j < 4; ++j) wa[j] = B_RING + (4 * g + q) * 256 + (((wc * 4 + j) ^ ((4 * g + q) & 7)) << 5) + 8 * p;
  f32x4 acc[4][4];
  bf16x8 hf[2][4], wf[2][4];
  auto seg = [&](int c, int ks, unsigned slot, bool doread, bool first = false) {
    bf16x4 lo[4];
#pragma unroll
    for (int m = 0; m < 16; ++m) {
      if (first) MFMA0(acc[m & 3][m >> 2], wf[c][m >> 2], hf[c][m & 3]);
      else MFMA(acc[m & 3][m >> 2], wf[c][m >> 2], hf[c][m & 3]);
      pin();
      if (doread && m < 12) {
        if (m >= 2 && m < 6) hf[c ^ 1][m - 2] = lds128(slot + ha[(m - 2) >> 1] + (m & 1) * 2048 + ks * 1024);
        else {
          const int jj = m < 2 ? 0 : (m - 4) >> 1;
          if ((m & 1) == 0) lo[jj] = ldstr(slot + wa[jj] + ks * 32 * 256);
          else wf[c ^ 1][jj] = cat(lo[jj], ldstr(slot + wa[jj] + (ks * 32 + 16) * 256));
        }
      }
      pin();
    }
  };
  waitl0();
  waitv<0>();
  STAMP(0, 3);
  bar();
  STAMP(1, 1);
#pragma unroll
  for (int i = 0; i < 4; ++i) hf[0][i] = lds128(ha[i >> 1] + (i & 1) * 2048);
#pragma unroll
  for (int j = 0; j < 4; ++j) wf[0][j] = cat(ldstr(wa[j]), ldstr(wa[j] + 16 * 256));
  pin();
#pragma unroll
  for (int r = 0; r < 6; ++r) {
    if (r == 3) {
      waitl0();
      if (lane < 4) *(LAS unsigned*)(4 * ST + ((lane & 1) * 8 + (lane >> 1)) * 1024 + wid * 4) = 1u;
      pin();
    }
    seg(r & 1, (r + 1) & 1, ((r + 1) >> 1) * ST, true, r == 0);
  }
  STAMP(0, 6);
  bar(); pin();
  STAMP(0, 7);
  seg(0, 1, 3 * ST, true);
  waitl0();
  bar();
  pin();
  auto tile = [&](int u) {
    seg(1, 0, (u % 5) * ST, true);
    seg(0, 1, (u % 5) * ST, true);
    waitl0();
    bar();
    pin();
  };
#pragma unroll
  for (int u = 0; u < (24 - TAILR) % 5; ++u) tile(4 + u);
  for (int it = 0; it < (24 - TAILR) / 5; ++it) {
#pragma unroll
    for (int u = 0; u < 5; ++u) tile(4 + (24 - TAILR) % 5 + u);
  }
  bf16x8 ta[2][4], tb[2][8];
  bf16x4 tl[8];
  u32x4 got[8];
  auto rd = [&](int c, int half, int T, int k) {
    const unsigned slot = (T % 5) * ST;
    const int ks = k / 10, r = k % 10;
    if (r == 2 || r == 3) ta[c][ks * 2 + r - 2] = lds128(slot + ha[half] + (r - 2) * 2048 + ks * 1024);
    else {
      const int jj = r < 2 ? 0 : (r - 2) >> 1;
      if ((r & 1) == 0) tl[ks * 4 + jj] = ldstr(slot + wa[jj] + ks * 32 * 256);
      else tb[c][ks * 4 + jj] = cat(tl[ks * 4 + jj], ldstr(slot + wa[jj] + (ks * 32 + 16) * 256));
    }
  };
  auto fetch = [&](int n) { got[n] = __builtin_amdgcn_raw_buffer_load_b128(rxc, mine + n * 1024, 0, 16); };
  pin();
#pragma unroll
  for (int m = 0; m < 16; ++m) {
    MFMA(acc[m & 3][m >> 2], wf[1][m >> 2], hf[1][m & 3]);
    pin();
    if (m < 10) { rd(0, 0, 28 - TAILR, 2 * m); rd(0, 0, 28 - TAILR, 2 * m + 1); }
    pin();
  }
#pragma unroll
  for (int n = 0; n < 2 * TAILR; ++n) {
    const int c = n & 1, half = n >= TAILR;
    if (n < TAILR - 1) { bar(); pin(); }
    if (n == TAILR) {
      STAMP(1, 3);
      asm volatile("" ::: "memory");
      pin();
    }
#pragma unroll
    for (int m = 0; m < 16; ++m) {
      MFMA(acc[2 * half + (m & 1)][(m >> 1) & 3], tb[c][m >> 1], ta[c][(m >> 3) * 2 + (m & 1)]);
      pin();
      if (n + 1 < 2 * TAILR && m < 10) {
        const int n1 = n + 1, h1 = n1 >= TAILR, T1 = 28 - TAILR + n1 - h1 * TAILR;
        rd(c ^ 1, h1, T1, 2 * m); rd(c ^ 1, h1, T1, 2 * m + 1);
      }
      if (n == TAILR && m >= 3 && m < 11) {
        const int e = m - 3;
        u32x4 v = __builtin_bit_cast(u32x4, acc[e & 1][e >> 1]);
#pragma unroll
        for (int z = 0; z < 4; ++z) v[z] = (v[z] & ~0xffu) | ((unsigned)(token >> (8 * z)) & 0xffu);
        __builtin_amdgcn_raw_buffer_store_b128(v, rxc, theirs + e * 1024, 0, 16);
      }
      if (n == TAILR + LDAT && m >= 8) fetch(m - 8);
      pin();
    }
  }
  pin(); asm volatile("s_nop 15" ::: "memory"); pin();
  STAMP(1, 2);
  if (LDAT >= TAILR) {
#pragma unroll
    for (int n = 0; n < 8; ++n) fetch(n);
  }
  for (unsigned spins = 0; spins < SPIN_MAX; ++spins) {
    unsigned bad = 0;
#pragma unroll
    for (int z = 0; z < 4; ++z) {
      unsigned a = got[0][z], o = a;
#pragma unroll
      for (int n = 1; n < 8; ++n) { a &= got[n][z]; o |= got[n][z]; }
      const unsigned t = (unsigned)(token >> (8 * z));
      bad |= (a ^ t) | (o ^ t);
    }
    if (__builtin_amdgcn_ballot_w64((bad & 0xffu) != 0) == 0) break;
    asm volatile("" ::: "memory");
#pragma unroll
    for (int n = 0; n < 8; ++n) fetch(n);
  }
  STAMP(1, 4);
  if (pn == 0 && wid == 0 && lane == 0) st_agent(ws + WS_EPOCH + pm * 128, token);
  const auto ro = __builtin_amdgcn_make_buffer_rsrc(out, 0, X_BYTES, 0x00020000);
  const unsigned ob = (pm * 128 + wr * 64 + kh * 32 + fr) * 2048 + (own * 128 + wc * 64 + (g & 1) * 16 + (g >> 1) * 8) * 2;
#pragma unroll
  for (int i = 0; i < 2; ++i)
#pragma unroll
    for (int jp = 0; jp < 2; ++jp) {
      unsigned w[2][2];
#pragma unroll
      for (int e = 0; e < 2; ++e) {
        const f32x4 s = acc[2 + i][2 * jp + e] + __builtin_bit_cast(f32x4, got[(2 * jp + e) * 2 + i] & ~0xffu);
        w[e][0] = pk(s[0], s[1]); w[e][1] = pk(s[2], s[3]);
      }
      const auto s0 = __builtin_amdgcn_permlane16_swap(w[0][0], w[1][0], false, false);
      const auto s1 = __builtin_amdgcn_permlane16_swap(w[0][1], w[1][1], false, false);
      __builtin_amdgcn_raw_buffer_store_b128((u32x4){s0[0], s1[0], s0[1], s1[1]}, ro, ob + i * 16 * 2048 + jp * 64, 0, OUTAUX);
    }
  waitv<0>();
  STAMP(1, 5);
  if (PROBE_AT != 4 && PROBE_AT != 5) { const u64 lim = tz + 6000 + PROBE_K * (st_r[PROBE_AT] - tz);
    while (__builtin_amdgcn_s_memrealtime() < lim) __builtin_amdgcn_s_sleep(1); }
#endif
}

extern "C" void kernel_launch(void* const* d_in, const int* in_sizes, int n_in, void* d_out, int out_size, void* d_ws, size_t ws_size,
                              hipStream_t stream) {
  (void)in_sizes; (void)n_in; (void)out_size; (void)ws_size;
  (void)hipFuncSetAttribute((const void*)mlp_kernel, hipFuncAttributeMaxDynamicSharedMemorySize, LDS_BYTES);
  mlp_kernel<<<256, 512, LDS_BYTES, stream>>>(d_in[0], d_in[1], d_in[2], (char*)d_ws, d_out);
}
```

```cpp
#include <hip/hip_runtime.h>
#include <hip/hip_bf16.h>

#define LAS __attribute__((address_space(3)))
#define GAS __attribute__((address_space(1)))
#define DI __device__ __forceinline__
typedef __bf16 bf16x8 __attribute__((ext_vector_type(8)));
typedef __bf16 bf16x4 __attribute__((ext_vector_type(4)));
typedef __bf16 bf16x2 __attribute__((ext_vector_type(2)));
typedef float f32x4 __attribute__((ext_vector_type(4)));
typedef float f32x2 __attribute__((ext_vector_type(2)));
typedef unsigned u32x4 __attribute__((ext_vector_type(4)));
typedef unsigned u32x2 __attribute__((ext_vector_type(2)));
typedef unsigned long long u64;

constexpr int M_ = 2048, D_ = 1024, F_ = 3584;
constexpr unsigned X_BYTES = M_ * D_ * 2, W_BYTES = D_ * F_ * 2, H_BYTES = M_ * F_ * 2;
constexpr unsigned XCH_BYTES = 256u * 32768u;
constexpr unsigned WS_H = 0, WS_XCH = H_BYTES;
constexpr unsigned WS_XFLAG = WS_XCH + XCH_BYTES;
constexpr unsigned WS_HFLAG = WS_XFLAG + 256 * 4 * 128;
constexpr unsigned WS_EPOCH = WS_HFLAG + 256 * 1024;

#define G1ROT (4 * (xcd >> 1) + 8 * (xcd & 1) + 2 * (lm >> 2) + 2 * (ln >> 1))
#define PFW2 9
#define ORDER 1
#define ORDSTART (2 * ln)
#define PFL2A -1
#define PFL2B -1
#define POLLDELAY 0
#define POLLSLEEP 1
#define FLAG1AT 0
#define FLAG2AT 2
#define PUBAUX 16
#define TAILR 5
#define LDAT 5
#define CLRAT 2
#define OUTAUX 16
constexpr unsigned G1_STAGE_B = 44 * 1024;
constexpr unsigned ST = 16384, LDS_BYTES = 10 * ST;
constexpr unsigned A_SLOTS = 0x21076, B_SLOTS = 0x85439;
__host__ __device__ constexpr unsigned aslot(int T) { return ((A_SLOTS >> (4 * (T % 5))) & 15) * ST; }
__host__ __device__ constexpr unsigned bslot(int T) { return ((B_SLOTS >> (4 * (T % 5))) & 15) * ST; }
#define FLAGN 70
#define PUBDELAY 0
#define PACE 1
#define PUB0 24
#define PUB1 30
#define PUB2 36
#define QDELAY 4
#define LV3AT 14
#define S6 1
constexpr unsigned SPIN_MAX = 1u << 16;

#if defined(__HIP_DEVICE_COMPILE__)
template <int N> DI void waitv() { asm volatile("s_waitcnt vmcnt(%0)" ::"n"(N) : "memory"); }
DI void waitl0() { asm volatile("" ::: "memory"); __builtin_amdgcn_s_waitcnt(0xc07f); asm volatile("" ::: "memory"); }
DI void bar() { asm volatile("" ::: "memory"); __builtin_amdgcn_s_barrier(); asm volatile("" ::: "memory"); }
DI void pin() { __builtin_amdgcn_sched_barrier(0); }
DI bf16x8 lds128(unsigned a) { return *(const LAS bf16x8*)a; }
DI bf16x4 ldstr(unsigned a) { return __builtin_amdgcn_ds_read_tr16_b64_v4bf16((LAS bf16x4*)a); }
DI bf16x8 cat(bf16x4 a, bf16x4 b) { return __builtin_shufflevector(a, b, 0, 1, 2, 3, 4, 5, 6, 7); }
DI unsigned pk(float lo, float hi) { f32x2 v = {lo, hi}; return __builtin_bit_cast(unsigned, __builtin_convertvector(v, bf16x2)); }
DI float relu(float v) { return __builtin_elementwise_maximum(v, 0.0f); }
DI u64 ld_agent(const char* p) { return __hip_atomic_load((GAS u64*)p, __ATOMIC_RELAXED, __HIP_MEMORY_SCOPE_AGENT); }
DI void st_agent(char* p, u64 v) { __hip_atomic_store((GAS u64*)p, v, __ATOMIC_RELAXED, __HIP_MEMORY_SCOPE_AGENT); }
DI u64 uni(u64 v) { return ((u64)(unsigned)__builtin_amdgcn_readfirstlane((unsigned)(v >> 32)) << 32) | (unsigned)__builtin_amdgcn_readfirstlane((unsigned)v); }
template <int N> struct ic { static constexpr int value = N; };
template <int N, int I = 0, class F> DI void static_for(F&& f) { if constexpr (I < N) { f(ic<I>{}); static_for<N, I + 1>(f); } }
#define MFMA(c, a, b) asm volatile("v_mfma_f32_16x16x32_bf16 %0, %1, %2, %0" : "+v"(c) : "v"(a), "v"(b))
#define MFMA0(c, a, b) asm volatile("v_mfma_f32_16x16x32_bf16 %0, %1, %2, 0" : "=&v"(c) : "v"(a), "v"(b))
#define DMA(rs, lds, vo, so, aux) __builtin_amdgcn_raw_ptr_buffer_load_lds(rs, (LAS void*)(lds), 16, vo, so, 0, aux)
#define STAMP(k, i) do {} while (0)
#define STAMP_DECL
#define STAMP_FLUSH(mask) do {} while (0)
#endif

__global__ __launch_bounds__(512, 2) void mlp_kernel(const void* __restrict__ x, const void* __restrict__ w1, const void* __restrict__ w2,
                                                     char* __restrict__ ws, void* __restrict__ out) {
#if defined(__HIP_DEVICE_COMPILE__)
  const int wid = __builtin_amdgcn_readfirstlane(threadIdx.x >> 6), lane = threadIdx.x & 63;
  const int bid = blockIdx.x, xcd = bid & 7, idx = bid >> 3;
  const int lm = idx & 7, ln = idx >> 3, pm = (xcd & 1) * 8 + lm, pn = (xcd >> 1) * 4 + ln, kh = pn >> 3, own = pn & 7;
  STAMP_DECL;
  STAMP(0, 0);
  if (wid >= 4) {
    const u64 epoch = ld_agent(ws + WS_EPOCH + pm * 128);
    const int lw = wid - 4;
    const auto rx = __builtin_amdgcn_make_buffer_rsrc((void*)x, 0, X_BYTES, 0x00020000);
    const auto rw1 = __builtin_amdgcn_make_buffer_rsrc((void*)w1, 0, W_BYTES, 0x00020000);
    const auto rw2 = __builtin_amdgcn_make_buffer_rsrc((void*)w2, 0, W_BYTES, 0x00020000);
    const auto rh = __builtin_amdgcn_make_buffer_rsrc((void*)(ws + WS_H), 0, H_BYTES, 0x00020000);
    u64 token;
    {
      const int rot = G1ROT;
      unsigned va[4], vb[7];
#pragma unroll
      for (int i = 0; i < 4; ++i) { const int row = (lw * 4 + i) * 8 + (lane >> 3); va[i] = (pm * 128 + row) * 2048 + (((lane & 7) ^ ((row >> 1) & 7)) << 4); }
#pragma unroll
      for (int i = 0; i < 7; ++i) { const int b = (lw * 7 + i) * 1024 + lane * 16, row = b / 448, cb = b - row * 448; vb[i] = row * 7168 + pn * 448 + (cb ^ (((row >> 3) & 1) << 5)); }
      auto issue = [&](int t, unsigned slot) {
        const unsigned kt = (t + rot) & 15, sa = kt * 128, sb = kt * (64 * 7168);
#pragma unroll
        for (int i = 0; i < 4; ++i) DMA(rx, slot + (lw * 4 + i) * 1024, va[i], sa, 0);
#pragma unroll
        for (int i = 0; i < 7; ++i) DMA(rw1, slot + 16384 + (lw * 7 + i) * 1024, vb[i], sb, 0);
      };
      issue(0, 0);
      issue(1, G1_STAGE_B);
      pin();
      token = uni(epoch) + 0x9e3779b97f4a7c15ull;
      asm volatile("" ::"s"(token));
      unsigned slot = 2 * G1_STAGE_B;
      waitv<11>();
      bar();
      for (int t = 0; t < 14; ++t) {
        if (t == PFW2) {
          const unsigned o = lane < 56 ? ((bid * 4 + lw) * 56 + lane) * 128 : 0xfffffff0u;
          __builtin_amdgcn_raw_ptr_buffer_load_lds(rw2, (LAS void*)(3 * G1_STAGE_B + lw * 256), 4, o, 0, 0, 0);
        }
        if (t == PFL2A || t == PFL2B) {
          const unsigned o = lane < 56 ? (kh * 1792 + idx * 56 + lw * 14 + (t == PFL2B) * 7 + (lane >> 3)) * 2048 + (own >> 2) * 1024 + (lane & 7) * 128 : 0xfffffff0u;
          __builtin_amdgcn_raw_ptr_buffer_load_lds(rw2, (LAS void*)(3 * G1_STAGE_B + lw * 256), 4, o, 0, 0, 0);
        }
        issue(t + 2, slot);
        slot = slot == 2 * G1_STAGE_B ? 0 : slot + G1_STAGE_B;
        waitv<11>();
        bar();
      }
    }
    auto slabinfo = [&](int s) {
      const int k = s < 21 ? (s - 7) >> 1 : s < 35 ? (s - 21) >> 1 : ((s - 35) * 86) >> 8;
      const int pr = s < 7 ? own : (own ^ (k + 1)) & 7;
      const int r = s < 7 ? s : s < 21 ? (s - 7) & 1 : s < 35 ? 2 + ((s - 21) & 1) : 4 + (s - 35) - 3 * k;
      return (unsigned)((pr * 7 + r) | (pr * 14 + (r < 6 ? 7 * (r & 1) + (r & ~1) : 6)) << 8 | (r < 6 ? 1 : 7) << 15 | pr * 8 << 18) | (s < 7 ? 0u : r < 2 ? 5u << r : r < 4 ? 0x10u : 0x20u) << 24;
    };
    auto lines = [&](unsigned e) { return (u64)(e >> 24) << ((e >> 18) & 63); };
    const char* fp = ws + WS_HFLAG + (pm * 16 + kh * 8) * 1024 + lane * 128;
    u64 ready = 0;
    auto ensure = [&](u64 need, u64 all) {
      if (__builtin_expect((ready & need) != need, 0)) {
        for (unsigned spins = 0;; ++spins) {
          bool hit = false;
          if (((all & ~ready) >> lane) & 1) hit = ld_agent(fp) == token;
          ready |= __builtin_amdgcn_ballot_w64(hit);
          if (spins >= SPIN_MAX) ready = ~0ull;
          if ((ready & need) == need) break;
          __builtin_amdgcn_s_sleep(POLLSLEEP);
        }
      }
      asm volatile("" ::: "memory");
    };
    if (lw < 2) {
      unsigned va[8], vb[4];
#pragma unroll
      for (int i = 0; i < 8; ++i) va[i] = ((pm * 8 + i) * 112 + kh * 56) * 1024 + lane * 16;
#pragma unroll
      for (int i = 0; i < 4; ++i) { const int row = i * 4 + (lane >> 4); vb[i] = (kh * 1792 + row) * 2048 + own * 256 + (((lane & 15) << 4) ^ ((row & 7) << 5)); }
      const unsigned tab = slabinfo(2 * lane + lw);
      auto entry = [&](int T) { return (unsigned)__builtin_amdgcn_readlane(tab, T); };
      auto issueA = [&](int T, unsigned slot) {
        const unsigned so = (entry(T) & 0xff) * 1024;
#pragma unroll
        for (int i = 0; i < 8; ++i) DMA(rh, slot + (i * 2 + lw) * 1024, va[i], so, 16);
      };
      auto issueB = [&](int T, unsigned slot) {
        const unsigned e = entry(T), so = ((e >> 8) & 0x7f) * (16 * 2048), s1 = so + ((e >> 15) & 7) * (16 * 2048);
#pragma unroll
        for (int i = 0; i < 8; ++i) DMA(rw2, slot + (lw * 8 + i) * 1024, vb[i & 3], i < 4 ? so : s1, 0);
      };
#pragma unroll
      for (int T = 0; T < 3; ++T) issueB(T, bslot(T));
      waitv<24>();
      bar();
      issueB(3, bslot(3));
      bar();
      const unsigned pw = aslot(4) + lw * 1024;
      if (lane < 2) *(LAS u32x4*)(pw + lane * 16) = (u32x4){0u, 0u, 0u, 0u};
      const u64 need0 = lines(entry(3)) | lines(entry(4)) | lines(entry(5));
      waitl0();
      waitv<0>();
      bar();
      u64 first = ~token;
      if ((need0 >> lane) & 1) first = ld_agent(fp);
      pin();
      issueB(4, bslot(4));
      pin();
      ready |= __builtin_amdgcn_ballot_w64(first == token);
      ensure(need0, need0);
      STAMP(0, 4);
      if (lw == 1) issueA(3, aslot(3));
      for (;;) {
        const u32x4 v = *(volatile const LAS u32x4*)pw;
        const unsigned v4 = *(volatile const LAS unsigned*)(pw + 16);
        if (__builtin_amdgcn_readfirstlane(v[0] & v[1] & v[2] & v[3] & v4)) break;
      }
      asm volatile("" ::: "memory");
      STAMP(0, 5);
      issueA(4, aslot(4));
      issueA(5, aslot(5));
      issueB(5, bslot(5));
      issueB(6, bslot(6));
      waitv<32>();
      bar();
      issueA(6, aslot(6));
      issueA(7, aslot(7)); issueB(7, bslot(7));
      waitv<48>();
      bar();
      unsigned sh = 12;
      for (int T = 8; T < 28; ++T) {
        issueA(T, ((A_SLOTS >> sh) & 15) * ST);
        issueB(T, ((B_SLOTS >> sh) & 15) * ST);
        sh = sh == 16 ? 0 : sh + 4;
        waitv<48>();
        bar();
      }
      waitv<32>();
      bar();
      waitv<16>();
      bar();
      waitv<0>();
      bar();
      if (lw == 1) STAMP_FLUSH(0x0030);
      return;
    }
    waitv<0>();
    bar();
    bar();
    if (lw == 2) {
      char* const myflag = ws + WS_HFLAG + (pm * 16 + pn) * 1024 + 512;
      unsigned hb[8];
#pragma unroll
      for (int i = 0; i < 8; ++i) hb[i] = ((pm * 8 + i) * 112 + pn * 7) * 1024 + lane * 16;
      unsigned la[2] = {lane * 16u + aslot(1), lane * 16u};
      asm volatile("" : "+v"(la[0]), "+v"(la[1]));
      bar();
      u32x4 hv[40];
#pragma unroll
      for (int n = 0; n < 40; ++n) {
        const int r = 2 + (n >> 3), i = n & 7;
        hv[n] = *(const LAS u32x4*)((r < 4 ? la[0] : la[1] + aslot(r >> 1)) + (i * 2 + (r & 1)) * 1024);
      }
      waitl0();
      if (lane < 2) *(LAS unsigned*)(aslot(4) + lane * 1024 + 16) = 1u;
      auto publish = [&](int n0, int n1) {
#pragma unroll
        for (int n = n0; n < n1; ++n) {
          __builtin_amdgcn_raw_buffer_store_b128(hv[n], rh, hb[n & 7], (2 + (n >> 3)) * 1024, 16);
          pin();
          if (PACE) __builtin_amdgcn_s_sleep(PACE);
          pin();
        }
      };
      publish(0, PUB0);
      bar();
      bar();
      publish(PUB0, PUB1);
      waitv<PUB1 - 16>();
      if (lane == 0) st_agent(myflag, token);
      bar();
      publish(PUB1, PUB2);
      bar();
      publish(PUB2, 40);
      bar();
      bar();
      bar();
      waitv<0>();
      if (lane == 0) st_agent(myflag + 128, token);
      return;
    }
    const unsigned t0 = slabinfo(2 * lane), t1 = slabinfo(2 * lane + 1);
    auto needof = [&](int T) { return lines(__builtin_amdgcn_readlane(t0, T)) | lines(__builtin_amdgcn_readlane(t1, T)); };
    const u64 others = ~(0xffull << (own * 8));
    const u64 lv1 = needof(6) | needof(7) | needof(8) | needof(9) | (needof(10) & 0x0f0f0f0f0f0f0f0full);
    const u64 lv2 = lv1 | (0x1010101010101010ull & others), lv3 = lv2 | (0x2020202020202020ull & others);
    bar();
    __builtin_amdgcn_s_sleep(QDELAY);
    ensure(needof(6) | needof(7), lv1);
    bar();
    for (int T = 8; T < 28; T += 2) {
      const u64 all = T < LV3AT ? lv2 : lv3;
      ensure(needof(T), all);
      u64 v = ~token;
      if (((all & ~ready) >> lane) & 1) v = ld_agent(fp);
      pin();
      bar();
      ensure(needof(T + 1), all);
      bar();
      pin();
      ready |= __builtin_amdgcn_ballot_w64(v == token);
      if ((ready & lv3) == lv3) break;
    }
    return;
  }
  __builtin_amdgcn_s_setprio(2);
  const int wr = wid >> 1, wc = wid & 1, fr = lane & 15, g = lane >> 4, q = fr >> 2, p = lane & 3;
  const u64 token = uni(ld_agent(ws + WS_EPOCH + pm * 128)) + 0x9e3779b97f4a7c15ull;
  asm volatile("" ::"s"(token));
  const auto rh = __builtin_amdgcn_make_buffer_rsrc((void*)(ws + WS_H), 0, H_BYTES, 0x00020000);
  const unsigned hg = ((pm * 8 + wr * 4) * 112 + pn * 7) * 1024 + lane * 16;
  const auto rxc = __builtin_amdgcn_make_buffer_rsrc((void*)(ws + WS_XCH), 0, XCH_BYTES, 0x00020000);
  const unsigned mine = (bid * 4 + wid) * 8192 + lane * 16, theirs = ((bid ^ 4) * 4 + wid) * 8192 + lane * 16;
  {
    unsigned xa[2], wa[2];
#pragma unroll
    for (int ks = 0; ks < 2; ++ks) xa[ks] = (wr * 64 + fr) * 128 + (((4 * ks + g) ^ (fr >> 1)) << 4);
#pragma unroll
    for (int e = 0; e < 2; ++e) wa[e] = 16384 + (8 * g + q) * 448 + wc * 224 + 8 * p + ((((wc + e) & 1) == 0) ? 32 * (g & 1) : -32 * (g & 1));
    unsigned xa2[2], wa2[2];
#pragma unroll
    for (int e = 0; e < 2; ++e) {
      xa2[e] = xa[e] + 2 * G1_STAGE_B; wa2[e] = wa[e] + 2 * G1_STAGE_B;
      asm volatile("" : "+v"(xa[e]), "+v"(wa[e]), "+v"(xa2[e]), "+v"(wa2[e]));
    }
    f32x4 acc[4][7];
#pragma unroll
    for (int i = 0; i < 4; ++i)
#pragma unroll
      for (int j = 0; j < 7; ++j) { acc[i][j] = (f32x4){0.f, 0.f, 0.f, 0.f}; asm volatile("" : "+v"(acc[i][j])); }
    bf16x8 xf[2][4], wf[2][7];
    auto seg = [&](int c, int ks, int st, bool doread) {
      bf16x4 lo[7];
      const unsigned xb = st < 2 ? xa[ks] + st * G1_STAGE_B : xa2[ks];
      const unsigned wb[2] = {st < 2 ? wa[0] + st * G1_STAGE_B : wa2[0], st < 2 ? wa[1] + st * G1_STAGE_B : wa2[1]};
#pragma unroll
      for (int m = 0; m < 28; ++m) {
        MFMA(acc[m & 3][m >> 2], wf[c][m >> 2], xf[c][m & 3]);
        pin();
        if (doread && m < 18) {
          if (m >= 2 && m < 6) xf[c ^ 1][m - 2] = lds128(xb + (m - 2) * 2048);
          else {
            const int jj = m < 2 ? 0 : (m - 4) >> 1;
            if ((m & 1) == 0) lo[jj] = ldstr(wb[jj & 1] + jj * 32 + ks * 32 * 448);
            else wf[c ^ 1][jj] = cat(lo[jj], ldstr(wb[jj & 1] + jj * 32 + (ks * 32 + 4) * 448));
          }
        }
        pin();
      }
    };
    bar();
    STAMP(0, 1);
#pragma unroll
    for (int i = 0; i < 4; ++i) xf[0][i] = lds128(xa[0] + i * 2048);
#pragma unroll
    for (int j = 0; j < 7; ++j) wf[0][j] = cat(ldstr(wa[j & 1] + j * 32), ldstr(wa[j & 1] + j * 32 + 4 * 448));
    seg(0, 1, 0, true);
    waitl0();
    bar();
    pin();
    auto tile = [&](int u) {
      seg(1, 0, u, true);
      seg(0, 1, u, true);
      waitl0();
      bar();
      pin();
    };
    tile(1); tile(2);
    for (int t = 0; t < 4; ++t) { tile(0); tile(1); tile(2); }
    STAMP(0, 2);
    unsigned hl[2] = {wr * 8192u + wc * 1024u + lane * 16u + aslot(0), wr * 8192u + wc * 1024u + lane * 16u};
    asm volatile("" : "+v"(hl[0]), "+v"(hl[1]));
    bf16x8 xt[2][4], wt[2][7];
    bf16x4 lo;
    u32x2 hp[4][7];
    float rl[4];
    auto rdw = [&](int ks, int j, int hi) {
      if (!hi) lo = ldstr(wa[j & 1] + j * 32 + ks * 32 * 448);
      else wt[ks][j] = cat(lo, ldstr(wa[j & 1] + j * 32 + (ks * 32 + 4) * 448));
    };
    auto fin = [&](int f) {
      const int b = f / 3, i = b & 3, j = b >> 2;
      const f32x4 v = acc[i][j];
      if (f % 3 == 0) { rl[0] = relu(v[0]); rl[1] = relu(v[1]); return; }
      if (f % 3 == 1) { hp[i][j][0] = pk(rl[0], rl[1]); rl[2] = relu(v[2]); return; }
      hp[i][j][1] = pk(rl[2], relu(v[3]));
      if (j == 1) __builtin_amdgcn_raw_buffer_store_b128((u32x4){hp[i][0][0], hp[i][0][1], hp[i][1][0], hp[i][1][1]}, rh, hg + wc * 1024 + i * 112 * 1024, 0, 16);
      *(LAS u32x2*)((j < 4 ? hl[0] + (aslot(j >> 1) - aslot(0)) + (j & 1) * 8 : j < 6 ? hl[1] + aslot(2) + (j & 1) * 8 : hl[1] - wc * 1016 + aslot(3)) + i * 2048) = hp[i][j];
    };
    static_for<84>([&](auto nc) {
      constexpr int n = decltype(nc)::value, qd = n >> 2, i = n & 3;
      constexpr int j = qd < 3 ? qd : qd < 9 ? (qd - 3) >> 1 : qd / 3, h = qd < 3 ? 0 : qd < 9 ? 1 + ((qd - 3) & 1) : qd % 3;
      if constexpr (n == 58) { waitl0(); bar(); pin(); }
      if constexpr (h == 0) MFMA(acc[i][j], wf[1][j], xf[1][i]);
      else MFMA(acc[i][j], wt[h - 1][j], xt[h - 1][i]);
      pin();
      if constexpr (n == 0 || n == 3) { rdw(n / 3, 0, 0); rdw(n / 3, 0, 1); }
      if constexpr (n == 1 || n == 2 || n == 4 || n == 5) {
        const int ks = n >> 2, e = ((n - 1) % 3) * 2;
        xt[ks][e] = lds128(xa[ks] + e * 2048); xt[ks][e + 1] = lds128(xa[ks] + (e + 1) * 2048);
      }
      if constexpr (n == 6 || n == 7) { rdw(n - 6, 1, 0); rdw(n - 6, 1, 1); }
      constexpr int rj = n < 31 ? 2 : n < 39 ? 3 : n < 45 ? 4 : n < 51 ? 5 : 6, r0 = rj == 2 ? 15 : rj == 3 ? 31 : 15 + 6 * rj;
      if constexpr (n >= r0 && n < r0 + 4) rdw((n - r0) >> 1, rj, (n - r0) & 1);
      constexpr int f0 = n < 19 ? 0 : n < 35 ? (n - 19) * 3 / 2 : n - 11, f1 = n < 19 ? 0 : n < 34 ? (n - 18) * 3 / 2 : n - 10;
#pragma unroll
      for (int f = f0; f < f1; ++f) fin(f);
      if constexpr (n == FLAGN) {
        waitv<0>();
        if (lane == 0) st_agent(ws + WS_HFLAG + (pm * 16 + pn) * 1024 + wid * 128, token);
      }
      pin();
    });
    fin(73); fin(74);
    pin();
    asm volatile("s_nop 3" ::: "memory");
    pin();
#pragma unroll
    for (int f = 75; f < 84; ++f) { fin(f); if (f % 3 == 2) pin(); }
  }
  unsigned hab[2][2], wab[2][4];
#pragma unroll
  for (int e = 0; e < 2; ++e) {
    hab[0][e] = wr * 8192 + (kh ^ e ^ 1) * 4096 + lane * 16; hab[1][e] = hab[0][e] + 6 * ST;
    asm volatile("" : "+v"(hab[0][e]), "+v"(hab[1][e]));
  }
#pragma unroll
  for (int j = 0; j < 4; ++j) {
    wab[0][j] = 3 * ST + (4 * g + q) * 256 + (((wc * 4 + j) ^ ((4 * g + q) & 7)) << 5) + 8 * p; wab[1][j] = wab[0][j] + 5 * ST;
    asm volatile("" : "+v"(wab[0][j]), "+v"(wab[1][j]));
  }
  auto ha = [&](int T, int e) { return aslot(T) < 6 * ST ? hab[0][e] + aslot(T) : hab[1][e] + (aslot(T) - 6 * ST); };
  auto wa = [&](int T, int j) { return bslot(T) < 8 * ST ? wab[0][j] + (bslot(T) - 3 * ST) : wab[1][j] + (bslot(T) - 8 * ST); };
  f32x4 acc[4][4];
  bf16x8 hf[2][4], wf[2][4];
  auto seg = [&](int c, int ks, int T, int doread, bool first = false) {
    bf16x4 lo[4];
#pragma unroll
    for (int m = 0; m < 16; ++m) {
      if (first) MFMA0(acc[m & 3][m >> 2], wf[c][m >> 2], hf[c][m & 3]);
      else MFMA(acc[m & 3][m >> 2], wf[c][m >> 2], hf[c][m & 3]);
      pin();
      if (doread && m < 12) {
        if (m >= 2 && m < 6) { if (doread == 1) hf[c ^ 1][m - 2] = lds128(ha(T, (m - 2) >> 1) + (m & 1) * 2048 + ks * 1024); }
        else {
          const int jj = m < 2 ? 0 : (m - 4) >> 1;
          if ((m & 1) == 0) lo[jj] = ldstr(wa(T, jj) + ks * 32 * 256);
          else wf[c ^ 1][jj] = cat(lo[jj], ldstr(wa(T, jj) + (ks * 32 + 16) * 256));
        }
      }
      pin();
    }
  };
  waitl0();
  STAMP(0, 3);
  bar();
  STAMP(1, 1);
#pragma unroll
  for (int i = 0; i < 4; ++i) hf[0][i] = lds128(ha(0, i >> 1) + (i & 1) * 2048);
#pragma unroll
  for (int j = 0; j < 4; ++j) wf[0][j] = cat(ldstr(wa(0, j)), ldstr(wa(0, j) + 16 * 256));
  pin();
#pragma unroll
  for (int r = 0; r < 6; ++r) {
    if (r == 3) {
      waitl0();
      if (lane < 2) *(LAS unsigned*)(aslot(4) + lane * 1024 + wid * 4) = 1u;
      pin();
    }
    seg(r & 1, (r + 1) & 1, (r + 1) >> 1, true, r == 0);
  }
  seg(0, 1, 3, 2);
  STAMP(0, 6);
  bar(); pin();
  STAMP(0, 7);
#pragma unroll
  for (int j = 0; j < 4; ++j) hf[1][j] = lds128(ha(3, j >> 1) + (j & 1) * 2048 + 1024);
  waitl0();
  bar();
  pin();
  auto tile = [&](int u) {
    seg(1, 0, u, true);
    seg(0, 1, u, true);
    waitl0();
    bar();
    pin();
  };
#pragma unroll
  for (int u = 0; u < (24 - TAILR) % 5; ++u) tile(4 + u);
  for (int it = 0; it < (24 - TAILR) / 5; ++it) {
#pragma unroll
    for (int u = 0; u < 5; ++u) tile(4 + (24 - TAILR) % 5 + u);
  }
  bf16x8 ta[2][4], tb[2][8];
  bf16x4 tl[8];
  u32x4 got[8];
  auto rd = [&](int c, int half, int T, int k) {
    const int ks = k / 10, r = k % 10;
    if (r == 2 || r == 3) ta[c][ks * 2 + r - 2] = lds128(ha(T, half) + (r - 2) * 2048 + ks * 1024);
    else {
      const int jj = r < 2 ? 0 : (r - 2) >> 1;
      if ((r & 1) == 0) tl[ks * 4 + jj] = ldstr(wa(T, jj) + ks * 32 * 256);
      else tb[c][ks * 4 + jj] = cat(tl[ks * 4 + jj], ldstr(wa(T, jj) + (ks * 32 + 16) * 256));
    }
  };
  auto fetch = [&](int n) { got[n] = __builtin_amdgcn_raw_buffer_load_b128(rxc, mine + n * 1024, 0, 16); };
  pin();
#pragma unroll
  for (int m = 0; m < 16; ++m) {
    MFMA(acc[m & 3][m >> 2], wf[1][m >> 2], hf[1][m & 3]);
    pin();
    if (m < 10) { rd(0, 0, 28 - TAILR, 2 * m); rd(0, 0, 28 - TAILR, 2 * m + 1); }
    pin();
  }
#pragma unroll
  for (int n = 0; n < 2 * TAILR; ++n) {
    const int c = n & 1, half = n >= TAILR;
    if (n < TAILR - 1) { bar(); pin(); }
    if (n == TAILR) {
      STAMP(1, 3);
      asm volatile("" ::: "memory");
      pin();
    }
#pragma unroll
    for (int m = 0; m < 16; ++m) {
      MFMA(acc[2 * half + (m & 1)][(m >> 1) & 3], tb[c][m >> 1], ta[c][(m >> 3) * 2 + (m & 1)]);
      pin();
      if (n + 1 < 2 * TAILR && m < 10) {
        const int n1 = n + 1, h1 = n1 >= TAILR, T1 = 28 - TAILR + n1 - h1 * TAILR;
        rd(c ^ 1, h1, T1, 2 * m); rd(c ^ 1, h1, T1, 2 * m + 1);
      }
      if (n == TAILR && m >= 3 && m < 11) {
        const int e = m - 3;
        u32x4 v = __builtin_bit_cast(u32x4, acc[e & 1][e >> 1]);
#pragma unroll
        for (int z = 0; z < 4; ++z) v[z] = (v[z] & ~0xffu) | ((unsigned)(token >> (8 * z)) & 0xffu);
        __builtin_amdgcn_raw_buffer_store_b128(v, rxc, theirs + e * 1024, 0, 16);
      }
      if (n == TAILR + LDAT && m >= 8) fetch(m - 8);
      pin();
    }
  }
  pin(); asm volatile("s_nop 15" ::: "memory"); pin();
  STAMP(1, 2);
  if (LDAT >= TAILR) {
#pragma unroll
    for (int n = 0; n < 8; ++n) fetch(n);
  }
  for (unsigned spins = 0; spins < SPIN_MAX; ++spins) {
    unsigned bad = 0;
#pragma unroll
    for (int z = 0; z < 4; ++z) {
      unsigned a = got[0][z], o = a;
#pragma unroll
      for (int n = 1; n < 8; ++n) { a &= got[n][z]; o |= got[n][z]; }
      const unsigned t = (unsigned)(token >> (8 * z));
      bad |= (a ^ t) | (o ^ t);
    }
    if (__builtin_amdgcn_ballot_w64((bad & 0xffu) != 0) == 0) break;
    asm volatile("" ::: "memory");
#pragma unroll
    for (int n = 0; n < 8; ++n) fetch(n);
  }
  STAMP(1, 4);
  if (pn == 0 && wid == 0 && lane == 0) st_agent(ws + WS_EPOCH + pm * 128, token);
  const auto ro = __builtin_amdgcn_make_buffer_rsrc(out, 0, X_BYTES, 0x00020000);
  const unsigned ob = (pm * 128 + wr * 64 + kh * 32 + fr) * 2048 + (own * 128 + wc * 64 + (g & 1) * 16 + (g >> 1) * 8) * 2;
#pragma unroll
  for (int i = 0; i < 2; ++i)
#pragma unroll
    for (int jp = 0; jp < 2; ++jp) {
      unsigned w[2][2];
#pragma unroll
      for (int e = 0; e < 2; ++e) {
        const f32x4 s = acc[2 + i][2 * jp + e] + __builtin_bit_cast(f32x4, got[(2 * jp + e) * 2 + i] & ~0xffu);
        w[e][0] = pk(s[0], s[1]); w[e][1] = pk(s[2], s[3]);
      }
      const auto s0 = __builtin_amdgcn_permlane16_swap(w[0][0], w[1][0], false, false);
      const auto s1 = __builtin_amdgcn_permlane16_swap(w[0][1], w[1][1], false, false);
      __builtin_amdgcn_raw_buffer_store_b128((u32x4){s0[0], s1[0], s0[1], s1[1]}, ro, ob + i * 16 * 2048 + jp * 64, 0, OUTAUX);
    }
#endif
}

extern "C" void kernel_launch(void* const* d_in, const int* in_sizes, int n_in, void* d_out, int out_size, void* d_ws, size_t ws_size,
                              hipStream_t stream) {
  (void)in_sizes; (void)n_in; (void)out_size; (void)ws_size;
  (void)hipFuncSetAttribute((const void*)mlp_kernel, hipFuncAttributeMaxDynamicSharedMemorySize, LDS_BYTES);
  mlp_kernel<<<256, 512, LDS_BYTES, stream>>>(d_in[0], d_in[1], d_in[2], (char*)d_ws, d_out);
}
```

```cpp
#define LV3AT 14
#define QDELAY 10
#include <hip/hip_runtime.h>
#include <hip/hip_bf16.h>

#define LAS __attribute__((address_space(3)))
#define GAS __attribute__((address_space(1)))
#define DI __device__ __forceinline__
typedef __bf16 bf16x8 __attribute__((ext_vector_type(8)));
typedef __bf16 bf16x4 __attribute__((ext_vector_type(4)));
typedef __bf16 bf16x2 __attribute__((ext_vector_type(2)));
typedef float f32x4 __attribute__((ext_vector_type(4)));
typedef float f32x2 __attribute__((ext_vector_type(2)));
typedef unsigned u32x4 __attribute__((ext_vector_type(4)));
typedef unsigned u32x2 __attribute__((ext_vector_type(2)));
typedef unsigned long long u64;

constexpr int M_ = 2048, D_ = 1024, F_ = 3584;
constexpr unsigned X_BYTES = M_ * D_ * 2, W_BYTES = D_ * F_ * 2, H_BYTES = M_ * F_ * 2;
constexpr unsigned XCH_BYTES = 256u * 32768u;
constexpr unsigned WS_H = 0, WS_XCH = H_BYTES;
constexpr unsigned WS_XFLAG = WS_XCH + XCH_BYTES;
constexpr unsigned WS_HFLAG = WS_XFLAG + 256 * 4 * 128;
constexpr unsigned WS_EPOCH = WS_HFLAG + 256 * 1024;

#define G1ROT (4 * (xcd >> 1) + 8 * (xcd & 1) + 2 * (lm >> 2) + 2 * (ln >> 1))
#define PFW2 9
#define ORDER 1
#define ORDSTART (2 * ln)
#define PFL2A -1
#define PFL2B -1
#define POLLDELAY 0
#define POLLSLEEP 1
#define FLAG1AT 0
#define FLAG2AT 2
#define PUBAUX 16
#define TAILR 5
#define LDAT 5
#define CLRAT 2
#define OUTAUX 16
constexpr unsigned G1_STAGE_B = 44 * 1024;
constexpr unsigned ST = 16384, LDS_BYTES = 10 * ST;
constexpr unsigned A_SLOTS = 0x21076, B_SLOTS = 0x85439;
__host__ __device__ constexpr unsigned aslot(int T) { return ((A_SLOTS >> (4 * (T % 5))) & 15) * ST; }
__host__ __device__ constexpr unsigned bslot(int T) { return ((B_SLOTS >> (4 * (T % 5))) & 15) * ST; }
#define FLAGN 70
#define PUBDELAY 0
#define PACE 1
#define PUB0 24
#define PACEK 2
#define PUBBARS 1
#define S6 1
constexpr unsigned SPIN_MAX = 1u << 20;

#if defined(__HIP_DEVICE_COMPILE__)
template <int N> DI void waitv() { asm volatile("s_waitcnt vmcnt(%0)" ::"n"(N) : "memory"); }
DI void waitl0() { asm volatile("" ::: "memory"); __builtin_amdgcn_s_waitcnt(0xc07f); asm volatile("" ::: "memory"); }
DI void bar() { asm volatile("" ::: "memory"); __builtin_amdgcn_s_barrier(); asm volatile("" ::: "memory"); }
DI void pin() { __builtin_amdgcn_sched_barrier(0); }
DI bf16x8 lds128(unsigned a) { return *(const LAS bf16x8*)a; }
DI bf16x4 ldstr(unsigned a) { return __builtin_amdgcn_ds_read_tr16_b64_v4bf16((LAS bf16x4*)a); }
DI bf16x8 cat(bf16x4 a, bf16x4 b) { return __builtin_shufflevector(a, b, 0, 1, 2, 3, 4, 5, 6, 7); }
DI unsigned pk(float lo, float hi) { f32x2 v = {lo, hi}; return __builtin_bit_cast(unsigned, __builtin_convertvector(v, bf16x2)); }
DI float relu(float v) { return __builtin_elementwise_maximum(v, 0.0f); }
DI u64 ld_agent(const char* p) { return __hip_atomic_load((GAS u64*)p, __ATOMIC_RELAXED, __HIP_MEMORY_SCOPE_AGENT); }
DI void st_agent(char* p, u64 v) { __hip_atomic_store((GAS u64*)p, v, __ATOMIC_RELAXED, __HIP_MEMORY_SCOPE_AGENT); }
DI u64 uni(u64 v) { return ((u64)(unsigned)__builtin_amdgcn_readfirstlane((unsigned)(v >> 32)) << 32) | (unsigned)__builtin_amdgcn_readfirstlane((unsigned)v); }
template <int N> struct ic { static constexpr int value = N; };
template <int N, int I = 0, class F> DI void static_for(F&& f) { if constexpr (I < N) { f(ic<I>{}); static_for<N, I + 1>(f); } }
#define MFMA(c, a, b) asm volatile("v_mfma_f32_16x16x32_bf16 %0, %1, %2, %0" : "+v"(c) : "v"(a), "v"(b))
#define MFMA0(c, a, b) asm volatile("v_mfma_f32_16x16x32_bf16 %0, %1, %2, 0" : "=&v"(c) : "v"(a), "v"(b))
#define DMA(rs, lds, vo, so, aux) __builtin_amdgcn_raw_ptr_buffer_load_lds(rs, (LAS void*)(lds), 16, vo, so, 0, aux)
#define STAMP(k, i) do {} while (0)
#define STAMP_DECL
#define STAMP_FLUSH(mask) do {} while (0)
#endif

__global__ __launch_bounds__(512, 2) void mlp_kernel(const void* __restrict__ x, const void* __restrict__ w1, const void* __restrict__ w2,
                                                     char* __restrict__ ws, void* __restrict__ out) {
#if defined(__HIP_DEVICE_COMPILE__)
  const int wid = __builtin_amdgcn_readfirstlane(threadIdx.x >> 6), lane = threadIdx.x & 63;
  const int bid = blockIdx.x, xcd = bid & 7, idx = bid >> 3;
  const int lm = idx & 7, ln = idx >> 3, pm = (xcd & 1) * 8 + lm, pn = (xcd >> 1) * 4 + ln, kh = pn >> 3, own = pn & 7;
  STAMP_DECL;
  STAMP(0, 0);
  if (wid >= 4) {
    const u64 epoch = ld_agent(ws + WS_EPOCH + pm * 128);
    const int lw = wid - 4;
    const auto rx = __builtin_amdgcn_make_buffer_rsrc((void*)x, 0, X_BYTES, 0x00020000);
    const auto rw1 = __builtin_amdgcn_make_buffer_rsrc((void*)w1, 0, W_BYTES, 0x00020000);
    const auto rw2 = __builtin_amdgcn_make_buffer_rsrc((void*)w2, 0, W_BYTES, 0x00020000);
    const auto rh = __builtin_amdgcn_make_buffer_rsrc((void*)(ws + WS_H), 0, H_BYTES, 0x00020000);
    auto slabinfo = [&](int s) {
      const int k = s < 21 ? (s - 7) >> 1 : s < 35 ? (s - 21) >> 1 : ((s - 35) * 86) >> 8;
      const int pr = s < 7 ? own : (own ^ (k + 1)) & 7;
      const int r = s < 7 ? s : s < 21 ? (s - 7) & 1 : s < 35 ? 2 + ((s - 21) & 1) : 4 + (s - 35) - 3 * k;
      return (unsigned)((pr * 7 + r) | (pr * 14 + (r < 6 ? 7 * (r & 1) + (r & ~1) : 6)) << 8 | (r < 6 ? 1 : 7) << 15 | pr * 8 << 18) | (s < 7 ? 0u : r < 2 ? 5u << r : r < 4 ? 0x10u : 0x20u) << 24;
    };
    u64 token;
    unsigned tab, tab0;
    {
      const int rot = G1ROT;
      unsigned va[4], vb[7];
#pragma unroll
      for (int i = 0; i < 4; ++i) { const int row = (lw * 4 + i) * 8 + (lane >> 3); va[i] = (pm * 128 + row) * 2048 + (((lane & 7) ^ ((row >> 1) & 7)) << 4); }
#pragma unroll
      for (int i = 0; i < 7; ++i) { const int b = (lw * 7 + i) * 1024 + lane * 16, row = b / 448, cb = b - row * 448; vb[i] = row * 7168 + pn * 448 + (cb ^ (((row >> 3) & 1) << 5)); }
      auto issue = [&](int t, unsigned slot) {
        const unsigned kt = (t + rot) & 15, sa = kt * 128, sb = kt * (64 * 7168);
#pragma unroll
        for (int i = 0; i < 4; ++i) DMA(rx, slot + (lw * 4 + i) * 1024, va[i], sa, 0);
#pragma unroll
        for (int i = 0; i < 7; ++i) DMA(rw1, slot + 16384 + (lw * 7 + i) * 1024, vb[i], sb, 0);
      };
      issue(0, 0);
      issue(1, G1_STAGE_B);
      pin();
      token = uni(epoch) + 0x9e3779b97f4a7c15ull;
      asm volatile("" ::"s"(token));
      tab = slabinfo(2 * lane + (lw & 1)); tab0 = slabinfo(2 * lane);
      asm volatile("" : "+v"(tab), "+v"(tab0));
      unsigned slot = 2 * G1_STAGE_B;
      waitv<11>();
      bar();
      for (int t = 0; t < 14; ++t) {
        if (t == PFW2) {
          const unsigned o = lane < 56 ? ((bid * 4 + lw) * 56 + lane) * 128 : 0xfffffff0u;
          __builtin_amdgcn_raw_ptr_buffer_load_lds(rw2, (LAS void*)(3 * G1_STAGE_B + lw * 256), 4, o, 0, 0, 0);
        }
        if (t == PFL2A || t == PFL2B) {
          const unsigned o = lane < 56 ? (kh * 1792 + idx * 56 + lw * 14 + (t == PFL2B) * 7 + (lane >> 3)) * 2048 + (own >> 2) * 1024 + (lane & 7) * 128 : 0xfffffff0u;
          __builtin_amdgcn_raw_ptr_buffer_load_lds(rw2, (LAS void*)(3 * G1_STAGE_B + lw * 256), 4, o, 0, 0, 0);
        }
        issue(t + 2, slot);
        slot = slot == 2 * G1_STAGE_B ? 0 : slot + G1_STAGE_B;
        waitv<11>();
        bar();
      }
    }
    auto lines = [&](unsigned e) { return (u64)(e >> 24) << ((e >> 18) & 63); };
    const char* fp = ws + WS_HFLAG + (pm * 16 + kh * 8) * 1024 + lane * 128;
    u64 ready = 0;
    auto ensure = [&](u64 need, u64 all) {
      if (__builtin_expect((ready & need) != need, 0)) {
        for (unsigned spins = 0;; ++spins) {
          bool hit = false;
          if (((all & ~ready) >> lane) & 1) hit = ld_agent(fp) == token;
          ready |= __builtin_amdgcn_ballot_w64(hit);
          if (spins >= SPIN_MAX) ready = ~0ull;
          if ((ready & need) == need) break;
          __builtin_amdgcn_s_sleep(POLLSLEEP);
        }
      }
      asm volatile("" ::: "memory");
    };
    if (lw < 2) {
      unsigned va[8], vb[4];
#pragma unroll
      for (int i = 0; i < 8; ++i) va[i] = ((pm * 8 + i) * 112 + kh * 56) * 1024 + lane * 16;
#pragma unroll
      for (int i = 0; i < 4; ++i) { const int row = i * 4 + (lane >> 4); vb[i] = (kh * 1792 + row) * 2048 + own * 256 + (((lane & 15) << 4) ^ ((row & 7) << 5)); }
      auto entry = [&](int T) { return (unsigned)__builtin_amdgcn_readlane(tab, T); };
      auto issueA = [&](int T, unsigned slot) {
        const unsigned so = (entry(T) & 0xff) * 1024;
#pragma unroll
        for (int i = 0; i < 8; ++i) DMA(rh, slot + (i * 2 + lw) * 1024, va[i], so, 16);
      };
      auto issueB = [&](int T, unsigned slot) {
        const unsigned e = entry(T), so = ((e >> 8) & 0x7f) * (16 * 2048), s1 = so + ((e >> 15) & 7) * (16 * 2048);
#pragma unroll
        for (int i = 0; i < 8; ++i) DMA(rw2, slot + (lw * 8 + i) * 1024, vb[i & 3], i < 4 ? so : s1, 0);
      };
#pragma unroll
      for (int T = 0; T < 3; ++T) issueB(T, bslot(T));
      waitv<24>();
      bar();
      issueB(3, bslot(3));
      bar();
      const unsigned pw = aslot(4) + lw * 1024;
      if (lane == 0) *(LAS u32x4*)pw = (u32x4){0u, 0u, 0u, 0u};
      const u64 need0 = lines(entry(3)) | lines(entry(4)) | lines(entry(5));
      waitl0();
      waitv<0>();
      bar();
      u64 first = ~token;
      if ((need0 >> lane) & 1) first = ld_agent(fp);
      pin();
      issueB(4, bslot(4));
      pin();
      ready |= __builtin_amdgcn_ballot_w64(first == token);
      ensure(need0, need0);
      STAMP(0, 4);
      if (lw == 1) issueA(3, aslot(3));
      for (;;) {
        const u32x4 v = *(volatile const LAS u32x4*)pw;
        if (__builtin_amdgcn_readfirstlane(v[0] & v[1] & v[2] & v[3])) break;
      }
      asm volatile("" ::: "memory");
      if (lw == 0 && lane == 0) st_agent(ws + WS_HFLAG + (pm * 16 + pn) * 1024 + 512, token);
      STAMP(0, 5);
      issueA(4, aslot(4));
      issueA(5, aslot(5));
      issueB(5, bslot(5));
      issueB(6, bslot(6));
      waitv<32>();
      bar();
      issueA(6, aslot(6));
      issueA(7, aslot(7)); issueB(7, bslot(7));
      waitv<48>();
      bar();
      unsigned sh = 12;
      for (int T = 8; T < 28; ++T) {
        issueA(T, ((A_SLOTS >> sh) & 15) * ST);
        issueB(T, ((B_SLOTS >> sh) & 15) * ST);
        sh = sh == 16 ? 0 : sh + 4;
        waitv<48>();
        bar();
      }
      waitv<32>();
      bar();
      waitv<16>();
      bar();
      waitv<0>();
      bar();
      if (lw == 1) STAMP_FLUSH(0x0030);
      return;
    }
    waitv<0>();
    if (lw == 2) {
      unsigned hb[8];
#pragma unroll
      for (int i = 0; i < 8; ++i) { hb[i] = ((pm * 8 + i) * 112 + pn * 7) * 1024 + lane * 16; asm volatile("" : "+v"(hb[i])); }
      bar();
      bar();
      bar();
      u32x4 hv[24];
#pragma unroll
      for (int n = 0; n < 24; ++n) hv[n] = *(const LAS u32x4*)(lane * 16 + aslot(2 + (n >> 4)) + ((n & 7) * 2 + ((n >> 3) & 1)) * 1024);
      pin();
      auto publish = [&](int n0, int n1) {
#pragma unroll
        for (int n = n0; n < n1; ++n) {
          __builtin_amdgcn_raw_buffer_store_b128(hv[n], rh, hb[n & 7], (4 + (n >> 3)) * 1024, 16);
          pin();
          if (PACE && n % PACEK == PACEK - 1) __builtin_amdgcn_s_sleep(PACE);
          pin();
        }
      };
      publish(0, PUB0);
      waitl0();
      bar();
      bar();
      publish(PUB0, 24);
#pragma unroll
      for (int k = 0; k < PUBBARS; ++k) bar();
      waitv<0>();
      if (lane == 0) st_agent(ws + WS_HFLAG + (pm * 16 + pn) * 1024 + 640, token);
      return;
    }
    auto needof = [&](int T) { return lines(__builtin_amdgcn_readlane(tab0, T)) | lines(__builtin_amdgcn_readlane(tab, T)); };
    const u64 others = ~(0xffull << (own * 8));
    const u64 lv1 = needof(6) | needof(7) | needof(8) | needof(9) | (needof(10) & 0x0f0f0f0f0f0f0f0full);
    const u64 lv2 = lv1 | (0x1010101010101010ull & others), lv3 = lv2 | (0x2020202020202020ull & others);
    const u64 need1 = needof(6) | needof(7);
    asm volatile("" ::"s"(lv1), "s"(need1));
    bar();
    bar();
    bar();
    __builtin_amdgcn_s_sleep(QDELAY);
    ensure(need1, lv1);
    bar();
    for (int T = 8; T < 28; T += 2) {
      const u64 all = T < LV3AT ? lv2 : lv3;
      ensure(needof(T), all);
      u64 v = ~token;
      if (((all & ~ready) >> lane) & 1) v = ld_agent(fp);
      pin();
      bar();
      ensure(needof(T + 1), all);
      bar();
      pin();
      ready |= __builtin_amdgcn_ballot_w64(v == token);
      if ((ready & lv3) == lv3) break;
    }
    return;
  }
  __builtin_amdgcn_s_setprio(2);
  const int wr = wid >> 1, wc = wid & 1, fr = lane & 15, g = lane >> 4, q = fr >> 2, p = lane & 3;
  const u64 token = uni(ld_agent(ws + WS_EPOCH + pm * 128)) + 0x9e3779b97f4a7c15ull;
  asm volatile("" ::"s"(token));
  const auto rh = __builtin_amdgcn_make_buffer_rsrc((void*)(ws + WS_H), 0, H_BYTES, 0x00020000);
  const unsigned hg = ((pm * 8 + wr * 4) * 112 + pn * 7 + wc) * 1024 + lane * 16;
  const auto rxc = __builtin_amdgcn_make_buffer_rsrc((void*)(ws + WS_XCH), 0, XCH_BYTES, 0x00020000);
  const unsigned mine = (bid * 4 + wid) * 8192 + lane * 16, theirs = ((bid ^ 4) * 4 + wid) * 8192 + lane * 16;
  {
    unsigned xa[2], wa[2];
#pragma unroll
    for (int ks = 0; ks < 2; ++ks) xa[ks] = (wr * 64 + fr) * 128 + (((4 * ks + g) ^ (fr >> 1)) << 4);
#pragma unroll
    for (int e = 0; e < 2; ++e) wa[e] = 16384 + (8 * g + q) * 448 + wc * 224 + 8 * p + ((((wc + e) & 1) == 0) ? 32 * (g & 1) : -32 * (g & 1));
    unsigned xa2[2], wa2[2];
#pragma unroll
    for (int e = 0; e < 2; ++e) {
      xa2[e] = xa[e] + 2 * G1_STAGE_B; wa2[e] = wa[e] + 2 * G1_STAGE_B;
      asm volatile("" : "+v"(xa[e]), "+v"(wa[e]), "+v"(xa2[e]), "+v"(wa2[e]));
    }
    f32x4 acc[4][7];
#pragma unroll
    for (int i = 0; i < 4; ++i)
#pragma unroll
      for (int j = 0; j < 7; ++j) { acc[i][j] = (f32x4){0.f, 0.f, 0.f, 0.f}; asm volatile("" : "+v"(acc[i][j])); }
    bf16x8 xf[2][4], wf[2][7];
    auto seg = [&](int c, int ks, int st, bool doread) {
      bf16x4 lo[7];
      const unsigned xb = st < 2 ? xa[ks] + st * G1_STAGE_B : xa2[ks];
      const unsigned wb[2] = {st < 2 ? wa[0] + st * G1_STAGE_B : wa2[0], st < 2 ? wa[1] + st * G1_STAGE_B : wa2[1]};
#pragma unroll
      for (int m = 0; m < 28; ++m) {
        MFMA(acc[m & 3][m >> 2], wf[c][m >> 2], xf[c][m & 3]);
        pin();
        if (doread && m < 18) {
          if (m >= 2 && m < 6) xf[c ^ 1][m - 2] = lds128(xb + (m - 2) * 2048);
          else {
            const int jj = m < 2 ? 0 : (m - 4) >> 1;
            if ((m & 1) == 0) lo[jj] = ldstr(wb[jj & 1] + jj * 32 + ks * 32 * 448);
            else wf[c ^ 1][jj] = cat(lo[jj], ldstr(wb[jj & 1] + jj * 32 + (ks * 32 + 4) * 448));
          }
        }
        pin();
      }
    };
    bar();
    STAMP(0, 1);
#pragma unroll
    for (int i = 0; i < 4; ++i) xf[0][i] = lds128(xa[0] + i * 2048);
#pragma unroll
    for (int j = 0; j < 7; ++j) wf[0][j] = cat(ldstr(wa[j & 1] + j * 32), ldstr(wa[j & 1] + j * 32 + 4 * 448));
    seg(0, 1, 0, true);
    waitl0();
    bar();
    pin();
    auto tile = [&](int u) {
      seg(1, 0, u, true);
      seg(0, 1, u, true);
      waitl0();
      bar();
      pin();
    };
    tile(1); tile(2);
    for (int t = 0; t < 4; ++t) { tile(0); tile(1); tile(2); }
    STAMP(0, 2);
    unsigned hl[2] = {wr * 8192u + wc * 1024u + lane * 16u + aslot(0), wr * 8192u + wc * 1024u + lane * 16u};
    asm volatile("" : "+v"(hl[0]), "+v"(hl[1]));
    bf16x8 xt[2][4], wt[2][7];
    bf16x4 lo;
    u32x2 hp[4][7];
    float rl[4];
    auto rdw = [&](int ks, int j, int hi) {
      if (!hi) lo = ldstr(wa[j & 1] + j * 32 + ks * 32 * 448);
      else wt[ks][j] = cat(lo, ldstr(wa[j & 1] + j * 32 + (ks * 32 + 4) * 448));
    };
    auto fin = [&](int f) {
      const int b = f / 3, i = b & 3, j = b >> 2;
      const f32x4 v = acc[i][j];
      if (f % 3 == 0) { rl[0] = relu(v[0]); rl[1] = relu(v[1]); return; }
      if (f % 3 == 1) { hp[i][j][0] = pk(rl[0], rl[1]); rl[2] = relu(v[2]); return; }
      hp[i][j][1] = pk(rl[2], relu(v[3]));
      if (j == 1 || j == 3) __builtin_amdgcn_raw_buffer_store_b128((u32x4){hp[i][j - 1][0], hp[i][j - 1][1], hp[i][j][0], hp[i][j][1]}, rh, hg + (j - 1) * 1024, i * 112 * 1024, 16);
      *(LAS u32x2*)((j < 4 ? hl[0] + (aslot(j >> 1) - aslot(0)) + (j & 1) * 8 : j < 6 ? hl[1] + aslot(2) + (j & 1) * 8 : hl[1] - wc * 1016 + aslot(3)) + i * 2048) = hp[i][j];
    };
    static_for<84>([&](auto nc) {
      constexpr int n = decltype(nc)::value, qd = n >> 2, i = n & 3;
      constexpr int j = qd < 3 ? qd : qd < 9 ? (qd - 3) >> 1 : qd / 3, h = qd < 3 ? 0 : qd < 9 ? 1 + ((qd - 3) & 1) : qd % 3;
      if constexpr (n == 58) { waitl0(); bar(); pin(); }
      if constexpr (h == 0) MFMA(acc[i][j], wf[1][j], xf[1][i]);
      else MFMA(acc[i][j], wt[h - 1][j], xt[h - 1][i]);
      pin();
      if constexpr (n == 0 || n == 3) { rdw(n / 3, 0, 0); rdw(n / 3, 0, 1); }
      if constexpr (n == 1 || n == 2 || n == 4 || n == 5) {
        const int ks = n >> 2, e = ((n - 1) % 3) * 2;
        xt[ks][e] = lds128(xa[ks] + e * 2048); xt[ks][e + 1] = lds128(xa[ks] + (e + 1) * 2048);
      }
      if constexpr (n == 6 || n == 7) { rdw(n - 6, 1, 0); rdw(n - 6, 1, 1); }
      constexpr int rj = n < 31 ? 2 : n < 39 ? 3 : n < 45 ? 4 : n < 51 ? 5 : 6, r0 = rj == 2 ? 15 : rj == 3 ? 31 : 15 + 6 * rj;
      if constexpr (n >= r0 && n < r0 + 4) rdw((n - r0) >> 1, rj, (n - r0) & 1);
      constexpr int f0 = n < 19 ? 0 : n < 35 ? (n - 19) * 3 / 2 : n - 11, f1 = n < 19 ? 0 : n < 34 ? (n - 18) * 3 / 2 : n - 10;
#pragma unroll
      for (int f = f0; f < f1; ++f) fin(f);
      if constexpr (n == FLAGN) {
        waitv<4>();
        if (lane == 0) st_agent(ws + WS_HFLAG + (pm * 16 + pn) * 1024 + wid * 128, token);
      }
      pin();
    });
    fin(73); fin(74);
    pin();
    asm volatile("s_nop 3" ::: "memory");
    pin();
#pragma unroll
    for (int f = 75; f < 84; ++f) { fin(f); if (f % 3 == 2) pin(); }
  }
  unsigned hab[2][2], wab[2][4];
#pragma unroll
  for (int e = 0; e < 2; ++e) {
    hab[0][e] = wr * 8192 + (kh ^ e ^ 1) * 4096 + lane * 16; hab[1][e] = hab[0][e] + 6 * ST;
    asm volatile("" : "+v"(hab[0][e]), "+v"(hab[1][e]));
  }
#pragma unroll
  for (int j = 0; j < 4; ++j) {
    wab[0][j] = 3 * ST + (4 * g + q) * 256 + (((wc * 4 + j) ^ ((4 * g + q) & 7)) << 5) + 8 * p; wab[1][j] = wab[0][j] + 5 * ST;
    asm volatile("" : "+v"(wab[0][j]), "+v"(wab[1][j]));
  }
  auto ha = [&](int T, int e) { return aslot(T) < 6 * ST ? hab[0][e] + aslot(T) : hab[1][e] + (aslot(T) - 6 * ST); };
  auto wa = [&](int T, int j) { return bslot(T) < 8 * ST ? wab[0][j] + (bslot(T) - 3 * ST) : wab[1][j] + (bslot(T) - 8 * ST); };
  f32x4 acc[4][4];
  bf16x8 hf[2][4], wf[2][4];
  auto seg = [&](int c, int ks, int T, int doread, bool first = false) {
    bf16x4 lo[4];
#pragma unroll
    for (int m = 0; m < 16; ++m) {
      if (first) MFMA0(acc[m & 3][m >> 2], wf[c][m >> 2], hf[c][m & 3]);
      else MFMA(acc[m & 3][m >> 2], wf[c][m >> 2], hf[c][m & 3]);
      pin();
      if (doread && m < 12) {
        if (m >= 2 && m < 6) { if (doread == 1) hf[c ^ 1][m - 2] = lds128(ha(T, (m - 2) >> 1) + (m & 1) * 2048 + ks * 1024); }
        else {
          const int jj = m < 2 ? 0 : (m - 4) >> 1;
          if ((m & 1) == 0) lo[jj] = ldstr(wa(T, jj) + ks * 32 * 256);
          else wf[c ^ 1][jj] = cat(lo[jj], ldstr(wa(T, jj) + (ks * 32 + 16) * 256));
        }
      }
      pin();
    }
  };
  waitl0();
  STAMP(0, 3);
  bar();
  STAMP(1, 1);
#pragma unroll
  for (int i = 0; i < 4; ++i) hf[0][i] = lds128(ha(0, i >> 1) + (i & 1) * 2048);
#pragma unroll
  for (int j = 0; j < 4; ++j) wf[0][j] = cat(ldstr(wa(0, j)), ldstr(wa(0, j) + 16 * 256));
  pin();
#pragma unroll
  for (int r = 0; r < 6; ++r) {
    if (r == 3) {
      waitl0();
      waitv<1>();
      if (lane < 2) *(LAS unsigned*)(aslot(4) + lane * 1024 + wid * 4) = 1u;
      pin();
    }
    seg(r & 1, (r + 1) & 1, (r + 1) >> 1, true, r == 0);
  }
  seg(0, 1, 3, 2);
  STAMP(0, 6);
  bar(); pin();
  STAMP(0, 7);
#pragma unroll
  for (int j = 0; j < 4; ++j) hf[1][j] = lds128(ha(3, j >> 1) + (j & 1) * 2048 + 1024);
  waitl0();
  bar();
  pin();
  auto tile = [&](int u) {
    seg(1, 0, u, true);
    seg(0, 1, u, true);
    waitl0();
    bar();
    pin();
  };
#pragma unroll
  for (int u = 0; u < (24 - TAILR) % 5; ++u) tile(4 + u);
  for (int it = 0; it < (24 - TAILR) / 5; ++it) {
#pragma unroll
    for (int u = 0; u < 5; ++u) tile(4 + (24 - TAILR) % 5 + u);
  }
  bf16x8 ta[2][4], tb[2][8];
  bf16x4 tl[8];
  u32x4 got[8];
  auto rd = [&](int c, int half, int T, int k) {
    const int ks = k / 10, r = k % 10;
    if (r == 2 || r == 3) ta[c][ks * 2 + r - 2] = lds128(ha(T, half) + (r - 2) * 2048 + ks * 1024);
    else {
      const int jj = r < 2 ? 0 : (r - 2) >> 1;
      if ((r & 1) == 0) tl[ks * 4 + jj] = ldstr(wa(T, jj) + ks * 32 * 256);
      else tb[c][ks * 4 + jj] = cat(tl[ks * 4 + jj], ldstr(wa(T, jj) + (ks * 32 + 16) * 256));
    }
  };
  auto fetch = [&](int n) { got[n] = __builtin_amdgcn_raw_buffer_load_b128(rxc, mine + n * 1024, 0, 16); };
  pin();
#pragma unroll
  for (int m = 0; m < 16; ++m) {
    MFMA(acc[m & 3][m >> 2], wf[1][m >> 2], hf[1][m & 3]);
    pin();
    if (m < 10) { rd(0, 0, 28 - TAILR, 2 * m); rd(0, 0, 28 - TAILR, 2 * m + 1); }
    pin();
  }
#pragma unroll
  for (int n = 0; n < 2 * TAILR; ++n) {
    const int c = n & 1, half = n >= TAILR;
    if (n < TAILR - 1) { bar(); pin(); }
    if (n == TAILR) {
      STAMP(1, 3);
      asm volatile("" ::: "memory");
      pin();
    }
#pragma unroll
    for (int m = 0; m < 16; ++m) {
      MFMA(acc[2 * half + (m & 1)][(m >> 1) & 3], tb[c][m >> 1], ta[c][(m >> 3) * 2 + (m & 1)]);
      pin();
      if (n + 1 < 2 * TAILR && m < 10) {
        const int n1 = n + 1, h1 = n1 >= TAILR, T1 = 28 - TAILR + n1 - h1 * TAILR;
        rd(c ^ 1, h1, T1, 2 * m); rd(c ^ 1, h1, T1, 2 * m + 1);
      }
      if (n == TAILR && m >= 3 && m < 11) {
        const int e = m - 3;
        u32x4 v = __builtin_bit_cast(u32x4, acc[e & 1][e >> 1]);
#pragma unroll
        for (int z = 0; z < 4; ++z) v[z] = (v[z] & ~0xffu) | ((unsigned)(token >> (8 * z)) & 0xffu);
        __builtin_amdgcn_raw_buffer_store_b128(v, rxc, theirs + e * 1024, 0, 16);
      }
      if (n == TAILR + LDAT && m >= 8) fetch(m - 8);
      pin();
    }
  }
  pin(); asm volatile("s_nop 15" ::: "memory"); pin();
  STAMP(1, 2);
  if (LDAT >= TAILR) {
#pragma unroll
    for (int n = 0; n < 8; ++n) fetch(n);
  }
  for (unsigned spins = 0; spins < SPIN_MAX; ++spins) {
    unsigned bad = 0;
#pragma unroll
    for (int z = 0; z < 4; ++z) {
      unsigned a = got[0][z], o = a;
#pragma unroll
      for (int n = 1; n < 8; ++n) { a &= got[n][z]; o |= got[n][z]; }
      const unsigned t = (unsigned)(token >> (8 * z));
      bad |= (a ^ t) | (o ^ t);
    }
    if (__builtin_amdgcn_ballot_w64((bad & 0xffu) != 0) == 0) break;
    asm volatile("" ::: "memory");
#pragma unroll
    for (int n = 0; n < 8; ++n) fetch(n);
  }
  STAMP(1, 4);
  if (pn == 0 && wid == 0 && lane == 0) st_agent(ws + WS_EPOCH + pm * 128, token);
  const auto ro = __builtin_amdgcn_make_buffer_rsrc(out, 0, X_BYTES, 0x00020000);
  const unsigned ob = (pm * 128 + wr * 64 + kh * 32 + fr) * 2048 + (own * 128 + wc * 64 + (g & 1) * 16 + (g >> 1) * 8) * 2;
#pragma unroll
  for (int i = 0; i < 2; ++i)
#pragma unroll
    for (int jp = 0; jp < 2; ++jp) {
      unsigned w[2][2];
#pragma unroll
      for (int e = 0; e < 2; ++e) {
        const f32x4 s = acc[2 + i][2 * jp + e] + __builtin_bit_cast(f32x4, got[(2 * jp + e) * 2 + i] & ~0xffu);
        w[e][0] = pk(s[0], s[1]); w[e][1] = pk(s[2], s[3]);
      }
      const auto s0 = __builtin_amdgcn_permlane16_swap(w[0][0], w[1][0], false, false);
      const auto s1 = __builtin_amdgcn_permlane16_swap(w[0][1], w[1][1], false, false);
      __builtin_amdgcn_raw_buffer_store_b128((u32x4){s0[0], s1[0], s0[1], s1[1]}, ro, ob + i * 16 * 2048 + jp * 64, 0, OUTAUX);
    }
#endif
}

extern "C" void kernel_launch(void* const* d_in, const int* in_sizes, int n_in, void* d_out, int out_size, void* d_ws, size_t ws_size,
                              hipStream_t stream) {
  (void)in_sizes; (void)n_in; (void)out_size; (void)ws_size;
  (void)hipFuncSetAttribute((const void*)mlp_kernel, hipFuncAttributeMaxDynamicSharedMemorySize, LDS_BYTES);
  mlp_kernel<<<256, 512, LDS_BYTES, stream>>>(d_in[0], d_in[1], d_in[2], (char*)d_ws, d_out);
}
```
